```python
import torch

def _softmax(x, axis=-1):
    return torch.softmax(x, dim=axis)
def _gelu(x):
    return torch.nn.functional.gelu(x, approximate="tanh")
def _silu(x):
    return torch.nn.functional.silu(x)
def _sigmoid(x):
    return torch.sigmoid(x)
def _layernorm(x, axis=-1, eps=1e-5):
    m = x.mean(dim=axis, keepdim=True)
    v = x.var(dim=axis, keepdim=True, unbiased=False)
    return (x - m) / torch.sqrt(v + eps)
def _scatter_add(tgt, idx, src, axis=0):
    return tgt.clone().index_add_(axis, idx, src)
def _segment_sum(src, seg, n):
    out = torch.zeros((n,) + src.shape[1:], dtype=src.dtype)
    return out.index_add_(0, seg, src)
def _pad(x, pw):
    flat = tuple(v for pair in reversed(pw) for v in pair)
    return torch.nn.functional.pad(x, flat)

kernel_name = 'recurrent_scan__linear_kernel_hedgehog__dense_activation_squared_relu__prompt'

def setup_inputs():

    _w = (lambda a: (lambda t: t.to(torch.bfloat16) if t.is_floating_point() else t)(torch.as_tensor(a)))
    def _A_setup():
        B, S, D, H = 1, 2048, 512, 32
        Dh = D // H
        return {
            "x": _w((lambda *s: torch.randn(*s, dtype=torch.bfloat16) * 0.05)(B, S, D)),
            "wqkv": _w((lambda *s: torch.randn(*s, dtype=torch.bfloat16) * 0.2)(D, 3 * D)),
            "w_phi": _w((lambda *s: torch.randn(*s, dtype=torch.bfloat16) * 0.2)(H, Dh, Dh) * (Dh ** -0.5)),
            "wo": _w((lambda *s: torch.randn(*s, dtype=torch.bfloat16) * 0.05)(D, D)),
        }
    _A_in = _A_setup()

    def _B_setup():
        B, S, D, F = 1, 2048, 512, 1792
        return {
            "x": _w((lambda *s: torch.randn(*s, dtype=torch.bfloat16) * 0.05)(B, S, D)),
            "w1": _w((lambda *s: torch.randn(*s, dtype=torch.bfloat16) * 0.05)(D, F)),
            "b1": _w((lambda *s: torch.randn(*s, dtype=torch.bfloat16) * 0.2)(F)),
            "w2": _w((lambda *s: torch.randn(*s, dtype=torch.bfloat16) * 0.05)(F, D)),
            "b2": _w((lambda *s: torch.randn(*s, dtype=torch.bfloat16) * 0.8)(D)),
        }
    _B_in = _B_setup()

    return {
        "A_x": _A_in["x"],
        "A_wqkv": _A_in["wqkv"],
        "A_w_phi": _A_in["w_phi"],
        "A_wo": _A_in["wo"],
        "B_x": _B_in["x"],
        "B_w1": _B_in["w1"],
        "B_b1": _B_in["b1"],
        "B_w2": _B_in["w2"],
        "B_b2": _B_in["b2"],
    }

def reference(A_x, A_wqkv, A_w_phi, A_wo, B_x, B_w1, B_b1, B_w2, B_b2):
    def _A_ref(x, wqkv, w_phi, wo):
        B, S, D = x.shape
        H = 32; Dh = D // H
        qkv = (lambda x, *d: x.permute(*d))((x @ wqkv).reshape(B, S, 3, H, Dh), 2, 0, 3, 1, 4)
        q, k, v = qkv[0], qkv[1], qkv[2]
        pq = torch.einsum("bhsd,hdf->bhsf", q, w_phi)
        pk = torch.einsum("bhsd,hdf->bhsf", k, w_phi)
        phi_q = torch.concatenate([_softmax(pq, axis=-1), _softmax(-pq, axis=-1)], axis=-1)
        phi_k = torch.concatenate([_softmax(pk, axis=-1), _softmax(-pk, axis=-1)], axis=-1)
        kv = torch.einsum("bhsf,bhsd->bhfd", phi_k, v)
        z  = torch.einsum("bhsf->bhf", phi_k)
        num = torch.einsum("bhqf,bhfd->bhqd", phi_q, kv)
        den = torch.einsum("bhqf,bhf->bhq", phi_q, z)[..., None] + 1e-6
        o = num / den
        return (lambda x, *d: x.permute(*d))(o, 0, 2, 1, 3).reshape(B, S, D) @ wo

    def _B_ref(x, w1, b1, w2, b2):
        h = torch.einsum("bsd,df->bsf", x, w1) + b1
        zero = torch.zeros_like(h)
        r = torch.where(h > zero, h, zero)
        h = r * r
        out = torch.einsum("bsf,fd->bsd", h, w2) + b2
        return out

    x = A_x
    xn = _layernorm(x, axis=-1)
    s = _A_ref(xn, A_wqkv, A_w_phi, A_wo)
    g = _silu(_B_ref(xn, B_w1, B_b1, B_w2, B_b2))
    return x + s * g
```

```cpp
#include <hip/hip_runtime.h>
#include <hip/hip_bf16.h>
typedef __hip_bfloat16 bf16;
constexpr int S=2048, D=512, H=32, DH=16, F=1792;
__device__ inline float b2f(bf16 v){ return __bfloat162float(v);}

__global__ void ln_k(const bf16* x, float* xn){
  int r=blockIdx.x; int t=threadIdx.x;
  float v[8]; float s=0;
  for(int i=0;i<8;i++){ v[i]=b2f(x[r*D+t*8+i]); s+=v[i]; }
  for(int o=32;o;o>>=1) s+=__shfl_xor(s,o);
  float m=s/D; float q=0;
  for(int i=0;i<8;i++){ float d=v[i]-m; q+=d*d; }
  for(int o=32;o;o>>=1) q+=__shfl_xor(q,o);
  float rs=1.0f/sqrtf(q/D+1e-5f);
  for(int i=0;i<8;i++) xn[r*D+t*8+i]=b2f(__float2bfloat16((v[i]-m)*rs));
}
__global__ void gemm_k(const float* A, const bf16* B, float* C, int N, int K){
  int r=blockIdx.y; int c=blockIdx.x*256+threadIdx.x; if(c>=N) return;
  float acc=0; for(int k=0;k<K;k++) acc+=A[r*K+k]*b2f(B[k*N+c]);
  C[r*N+c]=acc;
}
__global__ void phi_k(const float* qkv, const bf16* wphi, float* phiq, float* phik){
  int s=blockIdx.x; int t=threadIdx.x;
  int h=t&31, w=t>>5;
  const float* src=qkv+s*1536+w*512+h*16;
  float p[16];
  for(int f=0;f<16;f++){ float a=0; for(int d=0;d<16;d++) a+=b2f(__float2bfloat16(src[d]))*b2f(wphi[h*256+d*16+f]); p[f]=a; }
  float mx=-1e30f, mn=1e30f; for(int f=0;f<16;f++){ mx=fmaxf(mx,p[f]); mn=fminf(mn,p[f]); }
  float s1=0,s2=0; float e1[16],e2[16];
  for(int f=0;f<16;f++){ e1[f]=expf(p[f]-mx); e2[f]=expf(mn-p[f]); s1+=e1[f]; s2+=e2[f]; }
  float* dst=(w?phik:phiq)+(s*32+h)*32;
  for(int f=0;f<16;f++){ dst[f]=e1[f]/s1; dst[16+f]=e2[f]/s2; }
}
__global__ void kv_k(const float* phik, const float* qkv, float* kv){
  int h=blockIdx.x; int t=threadIdx.x; if(t>=544) return; int f=t/17, d=t%17;
  float a=0; for(int s=0;s<S;s++){ float pk=phik[(s*32+h)*32+f]; float v=d<16?qkv[s*1536+1024+h*16+d]:1.0f; a+=pk*v; }
  kv[h*544+t]=a;
}
__global__ void o_k(const float* phiq, const float* kv, float* o){
  int s=blockIdx.x; int t=threadIdx.x;
  int h=t>>4, d=t&15; float num=0, den=0;
  for(int f=0;f<32;f++){ float p=phiq[(s*32+h)*32+f]; num+=p*kv[h*544+f*17+d]; den+=p*kv[h*544+f*17+16]; }
  o[s*512+t]=b2f(__float2bfloat16(num/(den+1e-6f)));
}
__global__ void act_k(float* h, const bf16* b1){
  int i=blockIdx.x*256+threadIdx.x; float v=h[i]+b2f(b1[i%F]); v=v>0?v:0; h[i]=b2f(__float2bfloat16(v*v));
}
__global__ void fin_k(const bf16* x, const float* s, const float* m, const bf16* b2, bf16* out){
  int i=blockIdx.x*256+threadIdx.x; float g=m[i]+b2f(b2[i%D]); g=g/(1.0f+expf(-g)); out[i]=__float2bfloat16(b2f(x[i])+s[i]*g);
}
extern "C" void kernel_launch(void* const* d_in, const int* in_sizes, int n_in, void* d_out, int out_size, void* d_ws, size_t ws_size, hipStream_t stream){
  const bf16* x=(const bf16*)d_in[0]; const bf16* wqkv=(const bf16*)d_in[1]; const bf16* wphi=(const bf16*)d_in[2]; const bf16* wo=(const bf16*)d_in[3];
  const bf16* w1=(const bf16*)d_in[5]; const bf16* b1=(const bf16*)d_in[6]; const bf16* w2=(const bf16*)d_in[7]; const bf16* b2=(const bf16*)d_in[8];
  float* ws=(float*)d_ws;
  float* xn=ws; float* qkv=xn+S*D; float* phiq=qkv+S*1536; float* phik=phiq+S*1024; float* kv=phik+S*1024; float* o=kv+32*544; float* hh=o+S*D; float* sb=hh+S*F; float* mb=sb+S*D;
  ln_k<<<S,64,0,stream>>>(x,xn);
  gemm_k<<<dim3(6,S),256,0,stream>>>(xn,wqkv,qkv,1536,512);
  phi_k<<<S,64,0,stream>>>(qkv,wphi,phiq,phik);
  kv_k<<<32,576,0,stream>>>(phik,qkv,kv);
  o_k<<<S,512,0,stream>>>(phiq,kv,o);
  gemm_k<<<dim3(2,S),256,0,stream>>>(o,wo,sb,512,512);
  gemm_k<<<dim3(7,S),256,0,stream>>>(xn,w1,hh,F,512);
  act_k<<<S*F/256,256,0,stream>>>(hh,b1);
  gemm_k<<<dim3(2,S),256,0,stream>>>(hh,w2,mb,512,F);
  fin_k<<<S*D/256,256,0,stream>>>(x,sb,mb,b2,(bf16*)d_out);
}
```

```cpp
#include <hip/hip_runtime.h>
#include <hip/hip_bf16.h>
#include <stdint.h>

typedef short s4 __attribute__((ext_vector_type(4)));
typedef short s8 __attribute__((ext_vector_type(8)));
typedef float f2 __attribute__((ext_vector_type(2)));
typedef float f4 __attribute__((ext_vector_type(4)));
typedef float f16v __attribute__((ext_vector_type(16)));
typedef unsigned u4 __attribute__((ext_vector_type(4)));
typedef unsigned u2 __attribute__((ext_vector_type(2)));
typedef __bf16 bf2 __attribute__((ext_vector_type(2)));
typedef unsigned long long u64;
#define LAS __attribute__((address_space(3)))
#define GAS __attribute__((address_space(1)))
#define DI __device__ __forceinline__

constexpr unsigned WS_E    = 0;
constexpr unsigned WS_SEEN = 4096;
constexpr unsigned WS_F1   = 8192;
constexpr unsigned WS_F2   = 12288;
constexpr unsigned WS_FO   = 16384;
constexpr unsigned WS_FK   = 20480;
constexpr unsigned WS_ZP   = 32768;
constexpr unsigned WS_KVP  = 65536;
constexpr unsigned WS_HF   = 1048576;
constexpr unsigned WS_END  = WS_HF + 128u * 72u * 1024u;

constexpr unsigned L_MISC = 131072;
constexpr unsigned L_CS1  = L_MISC;
constexpr unsigned L_CS2  = L_MISC + 512;
constexpr unsigned L_B1   = L_MISC + 1024;
constexpr unsigned L_B2   = L_MISC + 2048;
constexpr unsigned L_WPHI = L_MISC + 3072;
constexpr unsigned L_ST   = L_MISC + 5120;
constexpr unsigned L_V    = L_MISC + 8192;
constexpr unsigned L_RED  = 32768;
constexpr unsigned L_PHI  = 12 * 8192;

#define PD 3
#define NPUB2 3
#define TPUB 4
#define QPUB 2
constexpr unsigned SPIN_CAP = 1u << 18;

DI unsigned pk2(float a, float b) { bf2 v = {(__bf16)a, (__bf16)b}; return __builtin_bit_cast(unsigned, v); }
DI float bflo(unsigned u) { return __uint_as_float(u << 16); }
DI float bfhi(unsigned u) { return __uint_as_float(u & 0xffff0000u); }
DI f4 mfma32(s8 a, s8 b, f4 c) { return __builtin_amdgcn_mfma_f32_16x16x32_bf16(a, b, c, 0, 0, 0); }
DI f16v mfmaW(s8 a, s8 b, f16v c) { return __builtin_amdgcn_mfma_f32_32x32x16_bf16(a, b, c, 0, 0, 0); }
DI s4 trr(unsigned addr) { return __builtin_amdgcn_ds_read_tr16_b64_v4i16((LAS s4*)addr); }
DI s8 trr2(unsigned a0, unsigned a1) { s4 x = trr(a0), y = trr(a1); return __builtin_shufflevector(x, y, 0, 1, 2, 3, 4, 5, 6, 7); }
DI s8 ldsr8(unsigned addr) { return *(LAS s8*)addr; }
DI f4 ldsrf4(unsigned addr) { return *(LAS f4*)addr; }
DI float dot2(unsigned a, unsigned b, float c) { return __builtin_amdgcn_fdot2_f32_bf16(__builtin_bit_cast(bf2, a), __builtin_bit_cast(bf2, b), c, false); }
DI float max3(float a, float b, float c) { return __builtin_elementwise_maximum(__builtin_elementwise_maximum(a, b), c); }
DI float xsum32(float t) {
  auto b = __builtin_amdgcn_permlane32_swap(__float_as_uint(t), __float_as_uint(t), false, false);
  return __uint_as_float(b[0]) + __uint_as_float(b[1]);
}

#define GLDS(voff, sbase, wb, LOFF)                                                                     \
  asm volatile("s_add_u32 m0, %2, %3\n\ts_nop 0\n\tglobal_load_lds_dwordx4 %0, %1" ::"v"(voff), "s"(sbase), \
               "s"(wb), "i"(LOFF) : "memory", "scc")
#define GLDS_S(voff, sbase, ldsaddr)                                                                    \
  asm volatile("s_mov_b32 m0, %2\n\ts_nop 0\n\tglobal_load_lds_dwordx4 %0, %1" ::"v"(voff), "s"(sbase), \
               "s"(ldsaddr) : "memory")
#define GLD4_SC1(dst, voff, sbase) asm volatile("global_load_dwordx4 %0, %1, %2 sc1" : "=v"(dst) : "v"(voff), "s"(sbase) : "memory")
#define GLD2(dst, voff, sbase) asm volatile("global_load_dwordx2 %0, %1, %2" : "=v"(dst) : "v"(voff), "s"(sbase) : "memory")
#define GLD2_SC1(dst, voff, sbase) asm volatile("global_load_dwordx2 %0, %1, %2 sc1" : "=v"(dst) : "v"(voff), "s"(sbase) : "memory")
#define WAIT_BAR(N) asm volatile("s_waitcnt vmcnt(%0) lgkmcnt(0)\n\ts_barrier" ::"i"(N) : "memory")
#define WAIT_VM(N) asm volatile("s_waitcnt vmcnt(%0)" ::"i"(N) : "memory")
#define LDS_BAR() asm volatile("s_waitcnt lgkmcnt(0)\n\ts_barrier" ::: "memory")
#define PIN(x) asm volatile("" : "+v"(x))
#define UNDEF(x) asm volatile("" : "=v"(x))
#define TOG(x) do { x ^= 65536u; PIN(x); } while (0)
#define SB() __builtin_amdgcn_sched_barrier(0)

#define TS(i)

DI void st_flag(char* ws, unsigned off, u64 tag) {
  u2 v = {(unsigned)tag, (unsigned)(tag >> 32)};
  asm volatile("s_mov_b64 exec, 1\n\tglobal_store_dwordx2 %0, %1, %2 sc1\n\ts_mov_b64 exec, -1" ::"v"(off), "v"(v), "s"(ws) : "memory");
}
DI void poll2(const char* ws, unsigned off0, int n0, unsigned off1, int n1, u64 tag, int lane) {
  unsigned off = off0 + (lane < n0 ? lane * 8 : 0);
  off = ((unsigned)(lane - 32) < (unsigned)n1) ? off1 + (lane - 32) * 8 : off;
#pragma nounroll
  for (unsigned spin = 0; spin < SPIN_CAP; ++spin) {
    u64 v = __hip_atomic_load((GAS u64*)(ws + off), __ATOMIC_RELAXED, __HIP_MEMORY_SCOPE_AGENT);
    if (__all(v == tag)) break;
    __builtin_amdgcn_s_sleep(1);
  }
}

constexpr int p2_ops(int n) { return ((n <= 9) ? 2 : 0) + ((n + PD <= 17) ? 2 : 0); }
constexpr int p2_wait(int n) {
  int c = 0;
  if (n < PD) { c = 2 * (PD - 1 - n); for (int i = 0; i < n; ++i) c += p2_ops(i); }
  else { for (int i = n - PD + 1; i < n; ++i) c += p2_ops(i); }
  if (n == NPUB2) { int p = 0; for (int i = 0; i < n; ++i) p += p2_ops(i); if (p < c) c = p; }
  return c;
}
constexpr int b2_row(int m) {
  return m < 16 ? (448 * (m >> 2) + ((m >> 1) & 1) * 320 + (m & 1) * 64)
       : m < 28 ? (448 * ((m - 16) / 3) + 128 + ((m - 16) % 3) * 64)
                : (m - 28) * 64;
}

struct Phi { u4 lo, hi; };
DI Phi feature_map(f4 acc0, f4 acc1, f4 cs, const float* rstd, const float* nm, s8 wphi) {
  float v0[4], v1[4];
#pragma unroll
  for (int e = 0; e < 4; ++e) { v0[e] = __builtin_fmaf(acc0[e], rstd[0], cs[e] * nm[0]); v1[e] = __builtin_fmaf(acc1[e], rstd[1], cs[e] * nm[1]); }
  auto sx = __builtin_amdgcn_permlane16_swap(pk2(v0[0], v0[1]), pk2(v1[0], v1[1]), false, false);
  auto sy = __builtin_amdgcn_permlane16_swap(pk2(v0[2], v0[3]), pk2(v1[2], v1[3]), false, false);
  u4 bfr = {sx[0], sy[0], sx[1], sy[1]};
  f16v z;
#pragma unroll
  for (int i = 0; i < 16; ++i) z[i] = 0.f;
  f16v p = mfmaW(wphi, __builtin_bit_cast(s8, bfr), z);
  float m0 = max3(p[0], p[1], p[2]), m1 = max3(p[3], p[4], p[5]), m2 = max3(p[6], p[7], p[8]), m3 = max3(p[9], p[10], p[11]), m4 = max3(p[12], p[13], p[14]);
  float mx = max3(max3(m0, m1, m2), max3(m3, m4, p[15]), p[15]);
  const float L2E = 1.4426950408889634f;
  const float ml = -mx * L2E;
  float ex[16];
#pragma unroll
  for (int i = 0; i < 16; ++i) ex[i] = __builtin_amdgcn_exp2f(__builtin_fmaf(p[i], L2E, ml));
  float s = (((ex[0] + ex[1]) + (ex[2] + ex[3])) + ((ex[4] + ex[5]) + (ex[6] + ex[7]))) + (((ex[8] + ex[9]) + (ex[10] + ex[11])) + ((ex[12] + ex[13]) + (ex[14] + ex[15])));
  float inv = __builtin_amdgcn_rcpf(s);
  Phi r;
#pragma unroll
  for (int i = 0; i < 4; ++i) { r.lo[i] = pk2(ex[2 * i] * inv, ex[2 * i + 1] * inv); r.hi[i] = pk2(ex[8 + 2 * i] * inv, ex[9 + 2 * i] * inv); }
  return r;
}
DI u2 h_tile(f4 acc, f4 cs, const float* b, float rstd, float nm) {
  float o[4];
#pragma unroll
  for (int e = 0; e < 4; ++e) {
    float v = __builtin_fmaf(acc[e], rstd, __builtin_fmaf(cs[e], nm, b[e]));
    v = fmaxf(v, 0.f);
    o[e] = v * v;
  }
  u2 r = {pk2(o[0], o[1]), pk2(o[2], o[3])};
  return r;
}
#define UNPK(arr, u) float arr[4] = {bflo(u[0]), bfhi(u[0]), bflo(u[1]), bfhi(u[1])}

__global__ void __launch_bounds__(512, 2)
fused_kernel(const char* __restrict__ x, const char* __restrict__ wqkv, const char* __restrict__ wphi,
             const char* __restrict__ wo, const char* __restrict__ w1, const char* __restrict__ b1,
             const char* __restrict__ w2, const char* __restrict__ b2, char* __restrict__ out, char* __restrict__ ws) {
  __shared__ __attribute__((aligned(1024))) char smem[163840];
  asm volatile("" ::"s"(x), "s"(wqkv), "s"(wphi), "s"(wo), "s"(w1), "s"(b1), "s"(w2), "s"(b2), "s"(out), "s"(ws));
  const int tid = threadIdx.x, l = tid & 63;
  const int w = __builtin_amdgcn_readfirstlane(tid >> 6);
  const int rg = w & 3, cg = w >> 2, r = l & 15, g = l >> 4;
  const int b = blockIdx.x, xcd = b & 7, bi = b >> 3;
  const int RG = (xcd >> 2) * 8 + (bi >> 2);
  const int typeA = (bi >> 1) & 1;
  const int slice = 2 * (xcd & 3) + (bi & 1);
  const int rot = bi >> 2;
  const int rt64 = xcd * 4 + (bi >> 3), ct = bi & 7;
  const unsigned sm0 = (unsigned)(uintptr_t)(LAS char*)smem;
  const unsigned wb = sm0 + w * 1024;
  TS(0);

  const unsigned voffX = (8 * w + (l >> 3)) * 1024 + (((l & 7) ^ ((4 * (w & 1) + (l >> 4)) & 7)) << 4);
  const int lam = w >> 2, tt_ = (w >> 1) & 1, gam = w & 1, eta = l >> 5, uu = (l >> 2) & 7;
  const int cidx = (l & 3) ^ (2 * (uu >> 2));
  const int krow1 = 16 * gam + 8 * (uu >> 2) + 4 * tt_ + (uu & 3);
  const unsigned rstr = typeA ? 3584u : 3072u;
  const unsigned voffB1 = krow1 * rstr + (typeA ? 128u : 1024u) * lam + (32 * eta + 8 * cidx) * 2;
  const char* xrow = x + (size_t)RG * 128 * 1024;
  const int odd = slice & 1;
  const char* wsel = typeA ? w1 : wqkv;
  const char* wb1 = wsel + (typeA ? (slice * 224 + odd * 96) * 2 : (512 + slice * 64) * 2);
  const unsigned kstr32 = 32u * rstr;

#define FILL_P1(J, I)                                                                                     \
  do {                                                                                                    \
    const int kc_ = ((J) + rot) & 7;                                                                      \
    if ((I) < 2) { GLDS(voffX, xrow + kc_ * 128 + (I) * 65536, wb, ((4 * (J) + (I)) & 15) * 8192); }      \
    else { GLDS(voffB1, wb1 + (size_t)(2 * kc_ + (I) - 2) * kstr32, wb, ((4 * (J) + (I)) & 15) * 8192); } \
  } while (0)
#define FILL_P1_ALL(J) do { FILL_P1(J, 0); FILL_P1(J, 1); FILL_P1(J, 2); FILL_P1(J, 3); } while (0)

  FILL_P1_ALL(0);
  u2 ep;
  { unsigned z0 = 0; GLD2_SC1(ep, z0, ws); }
  const unsigned l16 = l * 16;
  unsigned pf;
  {
    u64 pc; asm volatile("s_getpc_b64 %0" : "=s"(pc));
    const char* cb = (const char*)(pc & ~127ull) + (bi < 28 ? bi : 27) * 1024 + w * 128;
    unsigned vo = (l & 7) * 16;
    asm volatile("global_load_dword %0, %1, %2" : "=v"(pf) : "v"(vo), "s"(cb) : "memory");
  }
  {
    const unsigned vb1 = (unsigned)((slice * 224 + ((l & 16) ? (odd ? -32 : 128) : odd * 96)) * 2 + (l & 15) * 16);
    const unsigned vo = w == 0 ? vb1 : w == 3 ? (unsigned)(ct * 128 + (l & 7) * 16) : (unsigned)(slice * 2048 + (w & 1) * 1024) + l16;
    const char* src = w == 0 ? b1 : w == 3 ? b2 : wphi;
    const unsigned dst = sm0 + (w == 0 ? L_B1 : w == 3 ? L_B2 : L_WPHI + (w & 1) * 1024);
    if (w < 4) GLDS_S(vo, src, dst);
  }
  FILL_P1_ALL(1); FILL_P1_ALL(2); FILL_P1_ALL(3);

  const unsigned voffQ = (unsigned)((32 * lam + krow1) * 3072 + (32 * eta + 8 * cidx) * 2);
  const unsigned voffB2 = (unsigned)((32 * lam + 16 * tt_ + 8 * gam + uu) * 1024 + (ct * 64 + 32 * eta + 8 * cidx) * 2);
  const unsigned vP2a = typeA ? voffB1 : voffQ;
  const unsigned vP2b = typeA ? voffB1 : voffB2;
  const char* wb2 = wsel + (typeA ? (slice * 224 + (odd ? -32 : 128)) * 2 : (slice * 64) * 2);
  const char* wb3 = typeA ? wb2 : w2;
#define FILL_X(IDX)                                                                                               \
  do {                                                                                                            \
    const int i_ = (IDX) - 32;                                                                                    \
    const unsigned offA_ = (unsigned)(2 * (((i_ >> 1) + rot) & 7) + (i_ & 1)) * (32u * 3584u);                    \
    const unsigned offB_ = (IDX) < 40 ? (unsigned)((i_ + rot) & 7) * (64u * 3072u) : (unsigned)b2_row(((IDX) - 40) & 7) * 1024u; \
    const unsigned off_ = typeA ? offA_ : offB_;                                                                  \
    if ((IDX) < 40) GLDS(vP2a, wb2 + off_, wb, ((IDX) & 15) * 8192);                                              \
    else GLDS(vP2b, wb3 + off_, wb, ((IDX) & 15) * 8192);                                                         \
  } while (0)

  const int q = (l & 15) >> 2, p = l & 3;
  const unsigned base0_0 = sm0 + (g >> 1) * 1024 + (4 * (g & 1) + q) * 64 + 32 * (0 ^ (g & 1)) + 8 * p;
  const unsigned base0_1 = base0_0 ^ 32u;
  unsigned bB0 = base0_0 + cg * 512, bB1 = base0_1 + cg * 512;
  unsigned bCS = ((rg & 1) ? bB1 : bB0) + (rg >> 1) * 4096;
  unsigned bA[2][2];
#pragma unroll
  for (int i = 0; i < 2; ++i)
#pragma unroll
    for (int ks = 0; ks < 2; ++ks)
      bA[i][ks] = sm0 + (rg >> 1) * 8192 + (32 * (rg & 1) + 16 * (i ^ cg) + r) * 128 + (((4 * ks + g) ^ (r >> 1)) << 4);

  const s8 ones = {0x3F80, 0x3F80, 0x3F80, 0x3F80, 0x3F80, 0x3F80, 0x3F80, 0x3F80};
  const f4 fz = {0.f, 0.f, 0.f, 0.f};
  s8 A[2][16];
  s8 Bq[2][5];
  f4 acc[2][4] = {{fz, fz, fz, fz}, {fz, fz, fz, fz}};
  f4 cs = fz;
  float s1a = 0.f, s1b = 0.f, s2a = 0.f, s2b = 0.f;

  WAIT_BAR(12);
  TS(1);
  PIN(ep);
  const u64 tag = (((u64)__builtin_amdgcn_readfirstlane(ep[1]) << 32) | (unsigned)__builtin_amdgcn_readfirstlane(ep[0])) + 1ull;
#define RA(K) do { const unsigned sx_ = ((4 * ((K) >> 1)) & 7) * 8192; A[0][K] = ldsr8(bA[0][(K) & 1] + sx_); A[1][K] = ldsr8(bA[1][(K) & 1] + sx_); } while (0)
#define RB(K, T) do { const unsigned sb_ = ((4 * ((K) >> 1) + 2 + ((K) & 1)) & 7) * 8192;                              \
    const unsigned a_ = ((T) == 4 ? bCS : ((T) & 1) ? bB1 : bB0) + sb_ + ((T) == 2 || (T) == 3 ? 4096 : 0);            \
    Bq[(K) & 1][T] = trr2(a_, a_ + 2048); } while (0)
  RA(0); RB(0, 0); RB(0, 1); RB(0, 2); RB(0, 3); RB(0, 4);
  if (w == 0) st_flag(ws, WS_SEEN + b * 8, tag);
#pragma unroll
  for (int k = 0; k < 16; ++k) {
    const int c = k & 1, j = k >> 1;
    const bool fl = (k & 1) && k < 15;
    if (fl) {
      WAIT_BAR(8);
      if (j & 1) { TOG(bB0); TOG(bB1); TOG(bCS); TOG(bA[0][0]); TOG(bA[0][1]); TOG(bA[1][0]); TOG(bA[1][1]); }
    }
#define P1FILL(I) do { if (fl) { if (j <= 3) FILL_P1(j + 4, I); else FILL_X(32 + 4 * (j - 4) + (I)); } } while (0)
    u4 d = __builtin_bit_cast(u4, A[0][k]);
    acc[0][0] = mfma32(Bq[c][0], A[0][k], acc[0][0]); acc[1][0] = mfma32(Bq[c][0], A[1][k], acc[1][0]);
    if (k < 15) { RA(k + 1); RB(k + 1, 0); }
    SB();
    P1FILL(0);
    acc[0][1] = mfma32(Bq[c][1], A[0][k], acc[0][1]); acc[1][1] = mfma32(Bq[c][1], A[1][k], acc[1][1]);
    if (k < 15) { RB(k + 1, 1); RB(k + 1, 2); }
    SB();
    P1FILL(1);
    acc[0][2] = mfma32(Bq[c][2], A[0][k], acc[0][2]); acc[1][2] = mfma32(Bq[c][2], A[1][k], acc[1][2]);
    if (k < 15) { RB(k + 1, 3); RB(k + 1, 4); }
    SB();
    P1FILL(2);
    acc[0][3] = mfma32(Bq[c][3], A[0][k], acc[0][3]);
    s1a = dot2(d[0], 0x3F803F80u, s1a); s2a = dot2(d[0], d[0], s2a);
    acc[1][3] = mfma32(Bq[c][3], A[1][k], acc[1][3]);
    s1b = dot2(d[1], 0x3F803F80u, s1b); s2b = dot2(d[1], d[1], s2b);
    SB();
    P1FILL(3);
    cs = mfma32(Bq[c][4], ones, cs);
    s1a = dot2(d[2], 0x3F803F80u, s1a); s2a = dot2(d[2], d[2], s2a);
    s1b = dot2(d[3], 0x3F803F80u, s1b); s2b = dot2(d[3], d[3], s2b);
    SB();
  }
  TS(2);
  float rstd[2], nm[2];
  {
    auto sw = __builtin_amdgcn_permlane16_swap(__float_as_uint(s1a + s1b), __float_as_uint(s2a + s2b), false, false);
    float t = xsum32(__uint_as_float(sw[0]) + __uint_as_float(sw[1]));
    auto bc = __builtin_amdgcn_permlane16_swap(__float_as_uint(t), __float_as_uint(t), false, false);
    float m = __uint_as_float(bc[0]) * (1.0f / 512.0f);
    float v = __builtin_fmaf(__uint_as_float(bc[1]), 1.0f / 512.0f, -m * m);
    v = fmaxf(v, 0.f);
    rstd[0] = __builtin_amdgcn_rsqf(v + 1e-5f);
    nm[0] = -m * rstd[0];
    f2 st = {rstd[0], nm[0]};
    *(LAS f2*)(sm0 + L_ST + (w * 16 + r) * 8) = st;
  }
  *(LAS f4*)(sm0 + L_CS1 + ((4 * (rg >> 1) + 2 * cg + (rg & 1)) * 16 + 4 * g) * 4) = cs;
  TOG(bB0); TOG(bB1); TOG(bCS);
  WAIT_BAR(10);
  { f2 st = *(LAS f2*)(sm0 + L_ST + ((w ^ 4) * 16 + r) * 8); rstd[1] = st[0]; nm[1] = st[1]; }

  const __amdgpu_buffer_rsrc_t wsr = __builtin_amdgcn_make_buffer_rsrc(ws, 0, WS_END, 0x00020000);
  const int rtile0 = RG * 8 + rg * 2 + cg, rtile1 = RG * 8 + rg * 2 + (cg ^ 1);
  const unsigned hf0 = WS_HF + rtile0 * 72 * 1024, hf1 = WS_HF + rtile1 * 72 * 1024;
  const int RG2 = rt64 >> 1;
  const char* a2base = ws + WS_HF + (size_t)((rt64 * 4 + rg) * 72 + cg) * 1024;
  u4 A2[18][2];
  const unsigned sxor = typeA ? 0u : 65536u;
  const unsigned wlo = wb + sxor, whi = wb + (65536u - sxor);

#define FILL_B2(M)                                                                         \
  do {                                                                                     \
    const char* s_ = ((M) < 28 ? w2 : wo) + (size_t)b2_row(M) * 1024;                      \
    if (((M) & 15) < 8) GLDS(voffB2, s_, wlo, ((M) & 7) * 8192);                           \
    else GLDS(voffB2, s_, whi, ((M) & 7) * 8192);                                          \
  } while (0)
#define LOAD_A2(N, F) GLD4_SC1(A2[N][F], l16, a2base + (size_t)(4 * (N) + 2 * (F)) * 1024)

  f4 ap0, ap1, ap2, ap3, ap4, ap5;
  s8 wp0, wp1;
  UNDEF(wp0); UNDEF(wp1); UNDEF(ap0); UNDEF(ap1); UNDEF(ap2); UNDEF(ap3); UNDEF(ap4); UNDEF(ap5);
  const int pf_ = odd + 2 * cg, ph_ = odd + 1;
  int typeB = typeA ^ 1;
  asm volatile("" : "+s"(typeB));
  if (typeA) {
    FILL_X(44); FILL_X(45); FILL_X(46); FILL_X(47);
#pragma unroll
    for (int lm = 0; lm < 2; ++lm) {
      const unsigned ti = 4 * lm + 2 * cg;
      f4 c0 = ldsrf4(sm0 + L_CS1 + (ti * 16 + 4 * g) * 4), c1 = ldsrf4(sm0 + L_CS1 + ((ti + 1) * 16 + 4 * g) * 4);
      u2 bb0 = *(LAS u2*)(sm0 + L_B1 + (ti * 16 + 4 * g) * 2), bb1 = *(LAS u2*)(sm0 + L_B1 + ((ti + 1) * 16 + 4 * g) * 2);
      UNPK(bf0, bb0); UNPK(bf1, bb1);
#pragma unroll
      for (int i = 0; i < 2; ++i) {
        u2 lo = h_tile(acc[i][2 * lm], c0, bf0, rstd[i], nm[i]);
        u2 hi = h_tile(acc[i][2 * lm + 1], c1, bf1, rstd[i], nm[i]);
        u4 v = {lo[0], lo[1], hi[0], hi[1]};
        __builtin_amdgcn_raw_buffer_store_b128(v, wsr, l16 + (i ? hf1 : hf0) + (4 * slice + 2 * lm + cg) * 1024, 0, 16);
      }
    }
    TS(3);
    const unsigned oF = (pf_ >> 1) * 4096 + (pf_ & 1) * 512, oH = (ph_ >> 1) * 4096 + (ph_ & 1) * 512;
    unsigned bT[5] = {base0_0 + oF, base0_1 + oF, base0_0 + oH, base0_1 + oH, ((rg & 1) ? base0_1 : base0_0) + ((rg >> 1) ? oH : oF)};
#define RT(K, T) do { const unsigned a_ = bT[T] + ((K) & 7) * 8192; Bq[(K) & 1][T] = trr2(a_, a_ + 2048); } while (0)
    RT(0, 0); RT(0, 1); RT(0, 2); RT(0, 3); RT(0, 4);
    ap0 = fz; ap1 = fz; ap2 = fz; ap3 = fz; ap4 = fz; ap5 = fz; cs = fz;
    SB();
#pragma unroll
    for (int k = 0; k < 16; ++k) {
      const int c = k & 1, i = (k + 1) >> 1;
      const bool fl = (k & 1) && k < 15;
      if (fl) {
        WAIT_BAR(i == TPUB ? 2 * (TPUB - 1) : 16);
        if (i == TPUB && w == 0) st_flag(ws, WS_F1 + RG * 128 + slice * 8, tag);
        if (i == 4) { TOG(bT[0]); TOG(bT[1]); TOG(bT[2]); TOG(bT[3]); TOG(bT[4]); }
      }
      ap0 = mfma32(Bq[c][0], A[0][k], ap0); ap2 = mfma32(Bq[c][0], A[1][k], ap2);
      if (k < 15) { RT(k + 1, 0); RT(k + 1, 1); }
      SB();
      if (fl) FILL_B2(2 * (i - 1));
      ap1 = mfma32(Bq[c][1], A[0][k], ap1); ap3 = mfma32(Bq[c][1], A[1][k], ap3);
      if (k < 15) { RT(k + 1, 2); RT(k + 1, 3); }
      SB();
      if (fl) FILL_B2(2 * (i - 1) + 1);
      ap4 = mfma32(Bq[c][2], A[0][k], ap4); ap5 = mfma32(Bq[c][3], A[0][k], ap5);
      if (k < 15) { RT(k + 1, 4); }
      cs = mfma32(Bq[c][4], ones, cs);
      SB();
    }
#pragma unroll
    for (int i = 0; i < 2; ++i) {
#pragma unroll
      for (int k = 0; k < 16; ++k) UNDEF(A[i][k]);
#pragma unroll
      for (int k = 0; k < 4; ++k) UNDEF(acc[i][k]);
    }
  }
  if (typeB) {
#pragma unroll
    for (int c = 0; c < 2; ++c) {
      const unsigned a_ = sm0 + L_WPHI + (2 * cg + c) * 512 + (8 * (g >> 1) + q) * 32 + (2 * (g & 1) + (p >> 1)) * 8;
      u4 t = __builtin_bit_cast(u4, trr2(a_, a_ + 128));
      const unsigned sg = (l & 4) ? 0x80008000u : 0u;
      t[0] ^= sg; t[1] ^= sg; t[2] ^= sg; t[3] ^= sg;
      if (c) wp1 = __builtin_bit_cast(s8, t); else wp0 = __builtin_bit_cast(s8, t);
    }
    {
      const unsigned swz = (r >> 1) & 3;
      const unsigned prow = sm0 + L_PHI + (32 * rg + 16 * ((g & 1) ^ cg) + r) * 64;
      const unsigned pc0 = prow + (((2 * (g >> 1)) ^ swz) << 4), pc1 = prow + (((2 * (g >> 1) + 1) ^ swz) << 4);
#pragma unroll
      for (int c = 0; c < 2; ++c) {
        f4 ck = ldsrf4(sm0 + L_CS1 + ((2 * cg + c) * 16 + 4 * g) * 4);
        Phi ph = feature_map(acc[0][c], acc[1][c], ck, rstd, nm, c ? wp1 : wp0);
        *(LAS u4*)(pc0 + (2 * cg + c) * 8192) = ph.lo;
        *(LAS u4*)(pc1 + (2 * cg + c) * 8192) = ph.hi;
      }
      f4 cv0 = ldsrf4(sm0 + L_CS1 + ((4 + 2 * cg) * 16 + 4 * g) * 4), cv1 = ldsrf4(sm0 + L_CS1 + ((5 + 2 * cg) * 16 + 4 * g) * 4);
#pragma unroll
      for (int i = 0; i < 2; ++i) {
        float v0[4], v1[4];
#pragma unroll
        for (int e = 0; e < 4; ++e) { v0[e] = __builtin_fmaf(acc[i][2][e], rstd[i], cv0[e] * nm[i]); v1[e] = __builtin_fmaf(acc[i][3][e], rstd[i], cv1[e] * nm[i]); }
        u4 vv = {pk2(v0[0], v0[1]), pk2(v0[2], v0[3]), pk2(v1[0], v1[1]), pk2(v1[2], v1[3])};
        *(LAS u4*)(sm0 + L_V + cg * 8192 + (32 * rg + 16 * (i ^ cg) + r) * 64 + ((g ^ swz) << 4)) = vv;
      }
    }
    LDS_BAR();
    {
      const int hd = w >> 1, fh = w & 1;
      f4 kv = fz, zz = fz;
      const unsigned r0 = 4 * g + q;
      const unsigned swz = (r0 >> 1) & 3;
      const unsigned pa = sm0 + L_PHI + hd * 8192 + r0 * 64 + (((2 * fh + (p >> 1)) ^ swz) << 4) + 8 * (p & 1);
      const unsigned va = sm0 + L_V + (hd >> 1) * 8192 + r0 * 64 + ((p ^ swz) << 4) + 8 * (hd & 1);
#pragma unroll
      for (int R = 0; R < 4; ++R) {
        s8 pf8 = trr2(pa + R * 2048, pa + R * 2048 + 1024), vf8 = trr2(va + R * 2048, va + R * 2048 + 1024);
        kv = mfma32(pf8, vf8, kv);
        zz = mfma32(pf8, ones, zz);
      }
      u2 kvb = {pk2(kv[0], kv[1]), pk2(kv[2], kv[3])};
      u2 zb = {pk2(zz[0], zz[1]), pk2(zz[2], zz[3])};
      __builtin_amdgcn_raw_buffer_store_b64(kvb, wsr, l16 + WS_KVP + ((slice * 8 + w) * 8 + (RG >> 1)) * 1024 + 8 * (RG & 1), 0, 16);
      __builtin_amdgcn_raw_buffer_store_b64(zb, wsr, r == 0 ? (unsigned)((g >> 1) * 256 + 8 * (g & 1)) + WS_ZP + (slice * 8 + w) * 512 + RG * 16 : 0x7ffffff0u, 0, 16);
    }
    TS(3);
#define RQ(U, T) do { const unsigned a_ = ((T) == 4 ? bCS : ((T) & 1) ? bB1 : bB0) + (U) * 8192 + ((T) == 2 || (T) == 3 ? 4096 : 0); \
    Bq[(U) & 1][T] = trr2(a_, a_ + 2048); } while (0)
    RQ(0, 0); RQ(0, 1); RQ(0, 2); RQ(0, 3); RQ(0, 4);
    ap0 = fz; ap1 = fz; ap2 = fz; ap3 = fz; cs = fz;
    SB();
#pragma unroll
    for (int u = 0; u < 8; ++u) {
      const int c = u & 1, i = (u + 1) >> 1;
      const bool fl = (u & 1) && u < 7;
      if (fl) {
        WAIT_BAR(i == QPUB ? (QPUB == 1 ? 0 : 6 + 2 * (QPUB - 2)) : i == 1 ? 10 : 14);
        if (i == QPUB && w == 0) st_flag(ws, WS_FK + slice * 128 + RG * 8, tag);
      }
      ap0 = mfma32(Bq[c][0], A[0][2 * u], ap0); ap2 = mfma32(Bq[c][0], A[1][2 * u], ap2);
      if (u < 7) { RQ(u + 1, 0); RQ(u + 1, 1); }
      SB();
      if (fl && i == 1) { FILL_B2(4); FILL_B2(5); }
      ap1 = mfma32(Bq[c][1], A[0][2 * u], ap1); ap3 = mfma32(Bq[c][1], A[1][2 * u], ap3);
      if (u < 7) { RQ(u + 1, 2); RQ(u + 1, 3); }
      SB();
      if (fl && i == 1) { FILL_B2(6); FILL_B2(7); }
      ap0 = mfma32(Bq[c][2], A[0][2 * u + 1], ap0); ap2 = mfma32(Bq[c][2], A[1][2 * u + 1], ap2);
      if (u < 7) { RQ(u + 1, 4); }
      SB();
      if (fl) FILL_B2(8 + 2 * (i - 1));
      ap1 = mfma32(Bq[c][3], A[0][2 * u + 1], ap1); ap3 = mfma32(Bq[c][3], A[1][2 * u + 1], ap3);
      SB();
      if (fl) FILL_B2(9 + 2 * (i - 1));
      cs = mfma32(Bq[c][4], ones, cs);
      SB();
    }
    UNDEF(ap4); UNDEF(ap5);
  }
  TS(4);
  {
    const int ci = typeA ? 2 * ((rg >> 1) ? 1 : 2 * cg) + (rg & 1) : 4 * (rg >> 1) + 2 * cg + (rg & 1);
    *(LAS f4*)(sm0 + L_CS2 + (ci * 16 + 4 * g) * 4) = cs;
  }
  if (w == 0) poll2(ws, WS_F1 + RG2 * 128, 8, WS_FK + slice * 128, typeA ? 0 : 16, tag, l);
  WAIT_BAR(10);
  TS(5);
  u4 gp[8], zf;
#pragma unroll
  for (int i = 0; i < 8; ++i) UNDEF(gp[i]);
  UNDEF(zf);
  if (typeB) {
    const char* gb = ws + WS_KVP + (size_t)(slice * 8 + w) * 8192;
    const char* zb_ = ws + WS_ZP + (size_t)(slice * 8 + w) * 512;
    const unsigned zo = (l & 31) * 16;
    asm volatile("global_load_dwordx4 %0, %9, %10 sc1\n\tglobal_load_dwordx4 %1, %9, %10 offset:1024 sc1\n\t"
                 "global_load_dwordx4 %2, %9, %10 offset:2048 sc1\n\tglobal_load_dwordx4 %3, %9, %10 offset:3072 sc1\n\t"
                 "global_load_dwordx4 %4, %9, %11 sc1\n\tglobal_load_dwordx4 %5, %9, %11 offset:1024 sc1\n\t"
                 "global_load_dwordx4 %6, %9, %11 offset:2048 sc1\n\tglobal_load_dwordx4 %7, %9, %11 offset:3072 sc1\n\t"
                 "global_load_dwordx4 %8, %12, %13 sc1"
                 : "+v"(gp[0]), "+v"(gp[1]), "+v"(gp[2]), "+v"(gp[3]), "+v"(gp[4]), "+v"(gp[5]), "+v"(gp[6]), "+v"(gp[7]), "+v"(zf)
                 : "v"(l16), "s"(gb), "s"(gb + 4096), "v"(zo), "s"(zb_) : "memory");
  }
  FILL_B2(14); FILL_B2(15);
#pragma unroll
  for (int n = 0; n < PD; ++n) { LOAD_A2(n, 0); LOAD_A2(n, 1); }
  unsigned b2r[2][2];
#pragma unroll
  for (int el = 0; el < 2; ++el) {
    const unsigned o = sxor + cg * 4096 + (el ^ cg) * 512;
    b2r[el][0] = base0_0 + o; b2r[el][1] = base0_1 + o;
  }
  s8 W2[2][8];
#define RW(N, T) do { const unsigned a_ = b2r[((T) >> 1) & 1][(T) & 1] + ((2 * (N) + ((T) >> 2)) & 7) * 8192; W2[(N) & 1][T] = trr2(a_, a_ + 2048); } while (0)
  asm volatile("" : "+s"(typeB));
  if (typeA) {
    const unsigned uF = 2 * cg;
    f4 cF0 = ldsrf4(sm0 + L_CS2 + ((2 * uF) * 16 + 4 * g) * 4), cF1 = ldsrf4(sm0 + L_CS2 + ((2 * uF + 1) * 16 + 4 * g) * 4);
    f4 cH0 = ldsrf4(sm0 + L_CS2 + (2 * 16 + 4 * g) * 4), cH1 = ldsrf4(sm0 + L_CS2 + (3 * 16 + 4 * g) * 4);
    const unsigned bbase = sm0 + L_B1 + 256 + 8 * g;
    u2 bF0_ = *(LAS u2*)(bbase + (pf_ * 32) * 2), bF1_ = *(LAS u2*)(bbase + (pf_ * 32 + 16) * 2);
    u2 bH0_ = *(LAS u2*)(bbase + (ph_ * 32) * 2), bH1_ = *(LAS u2*)(bbase + (ph_ * 32 + 16) * 2);
    UNPK(fF0, bF0_); UNPK(fF1, bF1_); UNPK(fH0, bH0_); UNPK(fH1, bH1_);
#pragma unroll
    for (int i = 0; i < 2; ++i) {
      u2 lo = h_tile(i ? ap2 : ap0, cF0, fF0, rstd[i], nm[i]);
      u2 hi = h_tile(i ? ap3 : ap1, cF1, fF1, rstd[i], nm[i]);
      u4 v = {lo[0], lo[1], hi[0], hi[1]};
      __builtin_amdgcn_raw_buffer_store_b128(v, wsr, l16 + (i ? hf1 : hf0) + (32 + 3 * slice + uF) * 1024, 0, 16);
    }
    u2 lo = h_tile(ap4, cH0, fH0, rstd[0], nm[0]);
    u2 hi = h_tile(ap5, cH1, fH1, rstd[0], nm[0]);
    u4 v = {lo[0], lo[1], hi[0], hi[1]};
    __builtin_amdgcn_raw_buffer_store_b128(v, wsr, l16 + hf0 + (32 + 3 * slice + 1) * 1024, 0, 16);
  }
  if (typeB) {
    Phi phq[2];
#pragma unroll
    for (int c = 0; c < 2; ++c) {
      f4 ca = ldsrf4(sm0 + L_CS2 + ((2 * cg + c) * 16 + 4 * g) * 4), cb = ldsrf4(sm0 + L_CS2 + ((4 + 2 * cg + c) * 16 + 4 * g) * 4);
      phq[c] = feature_map(c ? ap1 : ap0, c ? ap3 : ap2, ca + cb, rstd, nm, c ? wp1 : wp0);
    }
    TS(9);
    SB();
    WAIT_VM(2 + 2 * PD);
#pragma unroll
    for (int i = 0; i < 8; ++i) PIN(gp[i]);
    PIN(zf);
    {
      float sm[4] = {0.f, 0.f, 0.f, 0.f};
      unsigned selLo = 0x00003F80u, selHi = 0x3F800000u;
      PIN(selLo); PIN(selHi);
#pragma unroll
      for (int i = 0; i < 8; ++i) {
        sm[0] = dot2(gp[i][0], selLo, sm[0]); sm[1] = dot2(gp[i][0], selHi, sm[1]);
        sm[2] = dot2(gp[i][1], selLo, sm[2]); sm[3] = dot2(gp[i][1], selHi, sm[3]);
        sm[0] = dot2(gp[i][2], selLo, sm[0]); sm[1] = dot2(gp[i][2], selHi, sm[1]);
        sm[2] = dot2(gp[i][3], selLo, sm[2]); sm[3] = dot2(gp[i][3], selHi, sm[3]);
      }
      u2 kb = {pk2(sm[0], sm[1]), pk2(sm[2], sm[3])};
      const unsigned ib = sm0 + L_V + (w >> 1) * 2048 + (w & 1) * 512;
      *(LAS u2*)(ib + (g >> 1) * 1024 + r * 16 + 8 * (g & 1)) = kb;
      *(LAS u4*)(ib + ((l >> 4) & 1) * 1024 + 256 + r * 16) = zf;
    }
    LDS_BAR();
    TS(10);
    {
      u4 o0, o1;
#pragma unroll
      for (int c = 0; c < 2; ++c) {
        s8 k0 = ldsr8(sm0 + L_V + (2 * cg + c) * 2048 + l16), k1 = ldsr8(sm0 + L_V + (2 * cg + c) * 2048 + 1024 + l16);
        f16v z;
#pragma unroll
        for (int i = 0; i < 16; ++i) z[i] = 0.f;
        z = mfmaW(k0, __builtin_bit_cast(s8, phq[c].lo), z);
        z = mfmaW(k1, __builtin_bit_cast(s8, phq[c].hi), z);
        float den = xsum32(((z[8] + z[9]) + (z[10] + z[11])) + ((z[12] + z[13]) + (z[14] + z[15]))) + 1e-6f;
        float id = __builtin_amdgcn_rcpf(den);
        o0[2 * c] = pk2(z[0] * id, z[1] * id); o0[2 * c + 1] = pk2(z[2] * id, z[3] * id);
        o1[2 * c] = pk2(z[4] * id, z[5] * id); o1[2 * c + 1] = pk2(z[6] * id, z[7] * id);
      }
      const unsigned oo = ((g & 1) ? hf1 : hf0) + (r + 16 * (g >> 1)) * 16;
      __builtin_amdgcn_raw_buffer_store_b128(o0, wsr, oo + (56 + 2 * slice + cg) * 1024, 0, 16);
      __builtin_amdgcn_raw_buffer_store_b128(o1, wsr, oo + (56 + 2 * slice + cg) * 1024 + 512, 0, 16);
    }
  }
  TS(6);

  f4 aM[4] = {fz, fz, fz, fz}, aS[4] = {fz, fz, fz, fz};
  u2 xr[2];
  const unsigned orow = (unsigned)((rt64 * 64 + rg * 16 + r) * 1024 + (ct * 64 + 32 * cg + 4 * g) * 2);
  RW(0, 0); RW(0, 1); RW(0, 2); RW(0, 3); RW(0, 4); RW(0, 5); RW(0, 6); RW(0, 7);
  SB();
#pragma unroll
  for (int n = 0; n < 18; ++n) {
    const int c = n & 1;
    if (n == 8 - PD && w == 0) poll2(ws, WS_F2 + RG2 * 128, 8, 0, 0, tag, l);
    if (n == 14 - PD && w == 0) {
      poll2(ws, WS_FO + RG2 * 128, 8, 0, 0, tag, l);
      if (b == 0) {
#pragma nounroll
        for (int cc = 0; cc < 4; ++cc) poll2(ws, WS_SEEN + cc * 512, 32, WS_SEEN + cc * 512 + 256, 32, tag, l);
        st_flag(ws, WS_E, tag);
      }
    }
    if (n < 17) WAIT_BAR(p2_wait(n)); else WAIT_VM(0);
    if (n == NPUB2 && w == 0) st_flag(ws, (typeA ? WS_F2 : WS_FO) + RG * 128 + slice * 8, tag);
    PIN(A2[n][0]); PIN(A2[n][1]);
    if ((n & 3) == 3) { TOG(b2r[0][0]); TOG(b2r[0][1]); TOG(b2r[1][0]); TOG(b2r[1][1]); }
    const s8 h0 = __builtin_bit_cast(s8, A2[n][0]), h1 = __builtin_bit_cast(s8, A2[n][1]);
#define MM(T, H) do { if (n < 14) aM[(T) & 3] = mfma32(W2[c][T], H, aM[(T) & 3]); else aS[(T) & 3] = mfma32(W2[c][T], H, aS[(T) & 3]); } while (0)
    MM(0, h0); MM(1, h0);
    if (n < 17) { RW(n + 1, 0); RW(n + 1, 1); RW(n + 1, 2); }
    SB();
    if (n <= 9) FILL_B2(2 * n + 16);
    MM(2, h0); MM(3, h0);
    if (n < 17) { RW(n + 1, 3); RW(n + 1, 4); RW(n + 1, 5); }
    SB();
    if (n <= 9) FILL_B2(2 * n + 17);
    MM(4, h1); MM(5, h1);
    if (n < 17) { RW(n + 1, 6); RW(n + 1, 7); }
    SB();
    if (n + PD <= 17) LOAD_A2(n + PD, 0);
    MM(6, h1);
    SB();
    if (n + PD <= 17) LOAD_A2(n + PD, 1);
    MM(7, h1);
    if (n == 12) { GLD2(xr[0], orow, x); GLD2(xr[1], orow, x + 32); }
    SB();
  }
  TS(7);
  {
    const unsigned wr = sm0 + L_RED + w * 4096 + l16;
    *(LAS f4*)(wr) = aM[2]; *(LAS f4*)(wr + 1024) = aM[3]; *(LAS f4*)(wr + 2048) = aS[2]; *(LAS f4*)(wr + 3072) = aS[3];
    LDS_BAR();
    PIN(xr[0]); PIN(xr[1]); PIN(pf);
    const unsigned rd = sm0 + L_RED + (w ^ 4) * 4096 + l16;
#pragma unroll
    for (int c = 0; c < 2; ++c) {
      f4 m = aM[c] + ldsrf4(rd + c * 1024);
      f4 s = aS[c] + ldsrf4(rd + 2048 + c * 1024);
      u2 bb = *(LAS u2*)(sm0 + L_B2 + (32 * cg + 16 * c + 4 * g) * 2);
      UNPK(bv, bb); UNPK(xv, xr[c]);
      float o[4];
#pragma unroll
      for (int e = 0; e < 4; ++e) {
        float t = m[e] + bv[e];
        float sg = __builtin_amdgcn_rcpf(1.0f + __builtin_amdgcn_exp2f(-1.4426950408889634f * t));
        o[e] = __builtin_fmaf(s[e], t * sg, xv[e]);
      }
      u2 ov = {pk2(o[0], o[1]), pk2(o[2], o[3])};
      *(GAS u2*)(out + orow + c * 32) = ov;
    }
  }
  TS(8);
}

extern "C" void kernel_launch(void* const* d_in, const int* in_sizes, int n_in, void* d_out, int out_size,
                              void* d_ws, size_t ws_size, hipStream_t stream) {
  fused_kernel<<<256, 512, 0, stream>>>((const char*)d_in[0], (const char*)d_in[1], (const char*)d_in[2],
                                        (const char*)d_in[3], (const char*)d_in[5], (const char*)d_in[6],
                                        (const char*)d_in[7], (const char*)d_in[8], (char*)d_out, (char*)d_ws);
}
```

```cpp
#include <hip/hip_runtime.h>
#include <hip/hip_bf16.h>
#include <stdint.h>

typedef short s4 __attribute__((ext_vector_type(4)));
typedef short s8 __attribute__((ext_vector_type(8)));
typedef float f2 __attribute__((ext_vector_type(2)));
typedef float f4 __attribute__((ext_vector_type(4)));
typedef float f16v __attribute__((ext_vector_type(16)));
typedef unsigned u4 __attribute__((ext_vector_type(4)));
typedef unsigned u2 __attribute__((ext_vector_type(2)));
typedef __bf16 bf2 __attribute__((ext_vector_type(2)));
typedef unsigned long long u64;
#define LAS __attribute__((address_space(3)))
#define GAS __attribute__((address_space(1)))
#define DI __device__ __forceinline__

constexpr unsigned WS_E    = 0;
constexpr unsigned WS_SEEN = 4096;
constexpr unsigned WS_F1   = 8192;
constexpr unsigned WS_F2   = 12288;
constexpr unsigned WS_FO   = 16384;
constexpr unsigned WS_FK   = 20480;
constexpr unsigned WS_ZP   = 32768;
constexpr unsigned WS_ST   = 655360;
constexpr unsigned WS_KVP  = 65536;
constexpr unsigned WS_HF   = 1048576;
constexpr unsigned WS_END  = WS_HF + 128u * 72u * 1024u;

constexpr unsigned L_MISC = 131072;
constexpr unsigned L_CS1  = L_MISC;
constexpr unsigned L_CS2  = L_MISC + 512;
constexpr unsigned L_B1   = L_MISC + 1024;
constexpr unsigned L_B2   = L_MISC + 2048;
constexpr unsigned L_WPHI = L_MISC + 3072;
constexpr unsigned L_ST   = L_MISC + 5120;
constexpr unsigned L_V    = L_MISC + 8192;
constexpr unsigned L_XR   = L_MISC + 24576;
constexpr unsigned L_RED  = 32768;
constexpr unsigned L_PHI  = 12 * 8192;

#define P1MODE 2
constexpr unsigned CODE_BYTES = 36u * 1024u;
constexpr unsigned SPIN_CAP = 1u << 18;

DI unsigned pk2(float a, float b) { bf2 v = {(__bf16)a, (__bf16)b}; return __builtin_bit_cast(unsigned, v); }
DI float bflo(unsigned u) { return __uint_as_float(u << 16); }
DI float bfhi(unsigned u) { return __uint_as_float(u & 0xffff0000u); }
DI f4 mfma32(s8 a, s8 b, f4 c) { return __builtin_amdgcn_mfma_f32_16x16x32_bf16(a, b, c, 0, 0, 0); }
DI f16v mfmaW(s8 a, s8 b, f16v c) { return __builtin_amdgcn_mfma_f32_32x32x16_bf16(a, b, c, 0, 0, 0); }
DI s4 trr(unsigned addr) { return __builtin_amdgcn_ds_read_tr16_b64_v4i16((LAS s4*)addr); }
DI s8 trr2(unsigned a0, unsigned a1) { s4 x = trr(a0), y = trr(a1); return __builtin_shufflevector(x, y, 0, 1, 2, 3, 4, 5, 6, 7); }
DI s8 ldsr8(unsigned addr) { return *(LAS s8*)addr; }
DI f4 ldsrf4(unsigned addr) { return *(LAS f4*)addr; }
DI float dot2(unsigned a, unsigned b, float c) { return __builtin_amdgcn_fdot2_f32_bf16(__builtin_bit_cast(bf2, a), __builtin_bit_cast(bf2, b), c, false); }
DI float max3(float a, float b, float c) { return __builtin_elementwise_maximum(__builtin_elementwise_maximum(a, b), c); }
DI float xsum32(float t) {
  auto b = __builtin_amdgcn_permlane32_swap(__float_as_uint(t), __float_as_uint(t), false, false);
  return __uint_as_float(b[0]) + __uint_as_float(b[1]);
}

#define GLDS(voff, sbase, wb, LOFF)                                                                     \
  asm volatile("s_add_u32 m0, %2, %3\n\ts_nop 0\n\tglobal_load_lds_dwordx4 %0, %1" ::"v"(voff), "s"(sbase), \
               "s"(wb), "i"(LOFF) : "memory", "scc")
#define GLDS_S(voff, sbase, ldsaddr)                                                                    \
  asm volatile("s_mov_b32 m0, %2\n\ts_nop 0\n\tglobal_load_lds_dwordx4 %0, %1" ::"v"(voff), "s"(sbase), \
               "s"(ldsaddr) : "memory")
#define GLD4_SC1(dst, voff, sbase) asm volatile("global_load_dwordx4 %0, %1, %2 sc1" : "=v"(dst) : "v"(voff), "s"(sbase) : "memory")
#define GLD2_SC1(dst, voff, sbase) asm volatile("global_load_dwordx2 %0, %1, %2 sc1" : "=v"(dst) : "v"(voff), "s"(sbase) : "memory")
#define WAIT_BAR(N) asm volatile("s_waitcnt vmcnt(%0) lgkmcnt(0)\n\ts_barrier" ::"i"(N) : "memory")
#define WAIT_VM(N) asm volatile("s_waitcnt vmcnt(%0)" ::"i"(N) : "memory")
#define LDS_BAR() asm volatile("s_waitcnt lgkmcnt(0)\n\ts_barrier" ::: "memory")
#define PIN(x) asm volatile("" : "+v"(x))
#define UNDEF(x) asm volatile("" : "=v"(x))
#define TOG(x) do { x ^= 65536u; PIN(x); } while (0)
#define SB() __builtin_amdgcn_sched_barrier(0)

DI void st_flag(char* ws, unsigned off, u64 tag) {
  u2 v = {(unsigned)tag, (unsigned)(tag >> 32)};
  asm volatile("s_mov_b64 exec, 1\n\tglobal_store_dwordx2 %0, %1, %2 sc1\n\ts_mov_b64 exec, -1" ::"v"(off), "v"(v), "s"(ws) : "memory");
}
DI void poll_slow(const char* ws, unsigned off, u64 tag) {
#pragma nounroll
  for (unsigned spin = 0; spin < SPIN_CAP; ++spin) {
    u64 v = __hip_atomic_load((GAS u64*)(ws + off), __ATOMIC_RELAXED, __HIP_MEMORY_SCOPE_AGENT);
    if (__all(v == tag)) break;
    __builtin_amdgcn_s_sleep(1);
  }
}
#define POLL_ISSUE(pv, voff) asm volatile("global_load_dwordx2 %0, %1, %2 sc1" : "+v"(pv) : "v"(voff), "s"(ws) : "memory")
#define POLL_CHECK(pv, voff) do { PIN(pv); if (!__all(pv[0] == (unsigned)tag && pv[1] == (unsigned)(tag >> 32))) poll_slow(ws, voff, tag); } while (0)

struct Phi { u4 lo, hi; };
DI u2 h_tile(f4 acc, f4 cs, const float* b, float rstd, float nm) {
  float o[4];
#pragma unroll
  for (int e = 0; e < 4; ++e) {
    float v = __builtin_fmaf(acc[e], rstd, __builtin_fmaf(cs[e], nm, b[e]));
    v = fmaxf(v, 0.f);
    o[e] = v * v;
  }
  u2 r = {pk2(o[0], o[1]), pk2(o[2], o[3])};
  return r;
}
#define UNPK(arr, u) float arr[4] = {bflo(u[0]), bfhi(u[0]), bflo(u[1]), bfhi(u[1])}

template <class Hook> DI void feature_map2(f4 a00, f4 a10, f4 cs0, s8 wp0, f4 a01, f4 a11, f4 cs1, s8 wp1, const float* rstd, const float* nm, Phi& o0, Phi& o1, Hook hook) {
  const float L2E = 1.4426950408889634f;
  f16v p[2];
#pragma unroll
  for (int h = 0; h < 2; ++h) {
    const f4 x0 = h ? a01 : a00, x1 = h ? a11 : a10, cs = h ? cs1 : cs0;
    float v0[4], v1[4];
#pragma unroll
    for (int e = 0; e < 4; ++e) { v0[e] = __builtin_fmaf(x0[e], rstd[0], cs[e] * nm[0]); v1[e] = __builtin_fmaf(x1[e], rstd[1], cs[e] * nm[1]); }
    auto sx = __builtin_amdgcn_permlane16_swap(pk2(v0[0], v0[1]), pk2(v1[0], v1[1]), false, false);
    auto sy = __builtin_amdgcn_permlane16_swap(pk2(v0[2], v0[3]), pk2(v1[2], v1[3]), false, false);
    u4 bfr = {sx[0], sy[0], sx[1], sy[1]};
    f16v z;
#pragma unroll
    for (int i = 0; i < 16; ++i) z[i] = 0.f;
    p[h] = mfmaW(h ? wp1 : wp0, __builtin_bit_cast(s8, bfr), z);
  }
  SB();
  hook(0);
  float m[2][4];
#pragma unroll
  for (int j = 0; j < 4; ++j)
#pragma unroll
    for (int h = 0; h < 2; ++h) { m[h][j] = max3(p[h][4 * j], p[h][4 * j + 1], p[h][4 * j + 2]); PIN(m[h][j]); }
  float ml[2];
#pragma unroll
  for (int h = 0; h < 2; ++h) { m[h][0] = max3(m[h][0], p[h][3], p[h][7]); m[h][2] = max3(m[h][2], p[h][11], p[h][15]); PIN(m[h][0]); PIN(m[h][2]); }
#pragma unroll
  for (int h = 0; h < 2; ++h) { m[h][0] = max3(m[h][0], m[h][1], m[h][2]); PIN(m[h][0]); }
#pragma unroll
  for (int h = 0; h < 2; ++h) ml[h] = __builtin_elementwise_maximum(m[h][0], m[h][3]) * -L2E;
  SB();
  hook(1);
#pragma unroll
  for (int i = 0; i < 16; ++i)
#pragma unroll
    for (int h = 0; h < 2; ++h) p[h][i] = __builtin_fmaf(p[h][i], L2E, ml[h]);
  SB();
  hook(2);
#pragma unroll
  for (int i = 0; i < 16; ++i)
#pragma unroll
    for (int h = 0; h < 2; ++h) p[h][i] = __builtin_amdgcn_exp2f(p[h][i]);
  SB();
  hook(3);
  float inv[2];
#pragma unroll
  for (int h = 0; h < 2; ++h) {
    float s = (((p[h][0] + p[h][1]) + (p[h][2] + p[h][3])) + ((p[h][4] + p[h][5]) + (p[h][6] + p[h][7]))) +
              (((p[h][8] + p[h][9]) + (p[h][10] + p[h][11])) + ((p[h][12] + p[h][13]) + (p[h][14] + p[h][15])));
    inv[h] = __builtin_amdgcn_rcpf(s);
  }
  SB();
#pragma unroll
  for (int i = 0; i < 4; ++i) {
    o0.lo[i] = pk2(p[0][2 * i] * inv[0], p[0][2 * i + 1] * inv[0]); o1.lo[i] = pk2(p[1][2 * i] * inv[1], p[1][2 * i + 1] * inv[1]);
    o0.hi[i] = pk2(p[0][8 + 2 * i] * inv[0], p[0][9 + 2 * i] * inv[0]); o1.hi[i] = pk2(p[1][8 + 2 * i] * inv[1], p[1][9 + 2 * i] * inv[1]);
  }
}
DI float row_sum64(float v) {
  v += __uint_as_float(__builtin_amdgcn_update_dpp(0, __float_as_uint(v), 0x121, 0xf, 0xf, false));
  v += __uint_as_float(__builtin_amdgcn_update_dpp(0, __float_as_uint(v), 0x122, 0xf, 0xf, false));
  v += __uint_as_float(__builtin_amdgcn_update_dpp(0, __float_as_uint(v), 0x124, 0xf, 0xf, false));
  v += __uint_as_float(__builtin_amdgcn_update_dpp(0, __float_as_uint(v), 0x128, 0xf, 0xf, false));
  auto a = __builtin_amdgcn_permlane16_swap(__float_as_uint(v), __float_as_uint(v), false, false);
  return xsum32(__uint_as_float(a[0]) + __uint_as_float(a[1]));
}
__global__ void __launch_bounds__(512, 2)
fused_kernel(const char* __restrict__ x, const char* __restrict__ wqkv, const char* __restrict__ wphi,
             const char* __restrict__ wo, const char* __restrict__ w1, const char* __restrict__ b1,
             const char* __restrict__ w2, const char* __restrict__ b2, char* __restrict__ out, char* __restrict__ ws) {
  __shared__ __attribute__((aligned(1024))) char smem[163840];
  asm volatile("" ::"s"(x), "s"(wqkv), "s"(wphi), "s"(wo), "s"(w1), "s"(b1), "s"(w2), "s"(b2), "s"(out), "s"(ws));
  const int tid = threadIdx.x, l = tid & 63;
  const int w = __builtin_amdgcn_readfirstlane(tid >> 6);
  const int rg = w & 3, cg = w >> 2, r = l & 15, g = l >> 4;
  const int b = blockIdx.x, xcd = b & 7, bi = b >> 3;
  const int RG = (xcd >> 2) * 8 + (bi >> 2);
  const int typeA = (bi >> 1) & 1;
  const int slice = 2 * (xcd & 3) + (bi & 1);
  const int rot = bi >> 2;
  const int rt64 = xcd * 4 + (bi >> 3), ct = bi & 7;
  const int rho1 = (ct + 2 * (bi >> 3)) & 7, rho2 = (ct + (bi >> 3)) & 3;
  const unsigned sm0 = (unsigned)(uintptr_t)(LAS char*)smem;
  const unsigned wb = sm0 + w * 1024;

  const unsigned voffX = (8 * w + (l >> 3)) * 1024 + (((l & 7) ^ ((4 * (w & 1) + (l >> 4)) & 7)) << 4);
  const int lam = w >> 2, tt_ = (w >> 1) & 1, gam = w & 1, eta = l >> 5, uu = (l >> 2) & 7;
  const int cidx = (l & 3) ^ (2 * (uu >> 2));
  const int krow1 = 16 * gam + 8 * (uu >> 2) + 4 * tt_ + (uu & 3);
  const unsigned rstr = typeA ? 3584u : 3072u;
  const unsigned voffB1 = krow1 * rstr + (typeA ? 128u : 1024u) * lam + (32 * eta + 8 * cidx) * 2;
  const char* xrow = x + (size_t)RG * 128 * 1024;
  const int odd = slice & 1;
  const char* wsel = typeA ? w1 : wqkv;
  const char* wb1 = wsel + (typeA ? (slice * 224 + odd * 96) * 2 : (512 + slice * 64) * 2);
  const unsigned kstr32 = 32u * rstr;

#define FILL_P1(J, I)                                                                                     \
  do {                                                                                                    \
    const int kc_ = ((J) + rot) & 7;                                                                      \
    if ((I) < 2) { GLDS(voffX, xrow + kc_ * 128 + (I) * 65536, wb, ((4 * (J) + (I)) & 15) * 8192); }      \
    else { GLDS(voffB1, wb1 + (size_t)(2 * kc_ + (I) - 2) * kstr32, wb, ((4 * (J) + (I)) & 15) * 8192); } \
  } while (0)
#define FILL_P1_ALL(J) do { FILL_P1(J, 0); FILL_P1(J, 1); FILL_P1(J, 2); FILL_P1(J, 3); } while (0)

  u2 ep;
  { unsigned z0 = 0; GLD2_SC1(ep, z0, ws); }
  u4 xs;
  const int srow = RG * 128 + (typeA * 8 + slice) * 8 + w;
  { const unsigned lo_ = l * 16; asm volatile("global_load_dwordx4 %0, %1, %2" : "=v"(xs) : "v"(lo_), "s"(x + (size_t)srow * 1024) : "memory"); }
  FILL_P1_ALL(0);
  asm volatile("s_barrier" ::: "memory");
  const unsigned l16 = l * 16;
  unsigned pf;
  {
    u64 pc; asm volatile("s_getpc_b64 %0" : "=s"(pc));
    const char* cb = (const char*)(pc & ~127ull);
    unsigned vo = (bi * 8 + w) * 128 + ((l & 1) ? 32768 : 0);
    vo = vo < CODE_BYTES - 128 ? vo : CODE_BYTES - 128;
    asm volatile("global_load_dword %0, %1, %2" : "=v"(pf) : "v"(vo), "s"(cb) : "memory");
  }
  {
    const unsigned vb1 = (unsigned)((slice * 224 + ((l & 16) ? (odd ? -32 : 128) : odd * 96)) * 2 + (l & 15) * 16);
    const unsigned vo = w == 0 ? vb1 : w == 3 ? (unsigned)(ct * 128 + (l & 7) * 16) : (unsigned)(slice * 2048 + (w & 1) * 1024) + l16;
    const char* src = w == 0 ? b1 : w == 3 ? b2 : wphi;
    const unsigned dst = sm0 + (w == 0 ? L_B1 : w == 3 ? L_B2 : L_WPHI + (w & 1) * 1024);
    if (w < 4) GLDS_S(vo, src, dst);
  }
  FILL_P1_ALL(1);

  const unsigned voffQ = (unsigned)((32 * lam + krow1) * 3072 + (32 * eta + 8 * cidx) * 2);
  const unsigned voffB2 = (unsigned)((32 * lam + 16 * tt_ + 8 * gam + uu) * 1024 + (ct * 64 + 32 * eta + 8 * cidx) * 2);
  const unsigned vP2b = typeA ? voffB1 : voffB2;
  const unsigned vP2a = typeA ? voffB1 : voffQ;
  const char* wb2 = wsel + (typeA ? (slice * 224 + (odd ? -32 : 128)) * 2 : (slice * 64) * 2);
  const char* wb3 = typeA ? wb2 : w2;
#define B2_ROW(M) ((M) < 16 ? 448 * (((((M) >> 1) + rho1) & 7) >> 1) + 320 * ((((M) >> 1) + rho1) & 1) + 64 * ((M) & 1) \
                 : (M) < 28 ? 448 * ((((M) - 16) / 3 + rho2) & 3) + 128 + 64 * (((M) - 16) % 3) : 64 * (((M) - 28 + rho1) & 7))
#define A2_KS(M) ((M) < 16 ? 4 * ((((M) >> 1) + rho1) & 7) + 2 * ((M) & 1) \
                : (M) < 28 ? 32 + 6 * ((((M) - 16) / 3 + rho2) & 3) + 2 * (((M) - 16) % 3) : 56 + 2 * (((M) - 28 + rho1) & 7))
#define FILL_X(IDX)                                                                                               \
  do {                                                                                                            \
    const int i_ = (IDX) - 32;                                                                                    \
    const unsigned offA_ = (unsigned)(2 * (((i_ >> 1) + rot) & 7) + (i_ & 1)) * (32u * 3584u);                    \
    const unsigned offB_ = (IDX) < 36 ? (unsigned)B2_ROW(i_ & 3) * 1024u : (unsigned)((i_ - 4 + rot) & 7) * (64u * 3072u); \
    const unsigned off_ = typeA ? offA_ : offB_;                                                                  \
    if ((IDX) < 36) GLDS(vP2b, wb3 + off_, wb, ((IDX) & 15) * 8192);                                              \
    else GLDS(vP2a, wb2 + off_, wb, ((IDX) & 15) * 8192);                                                         \
  } while (0)
#define FILL_B2(M) GLDS(voffB2, ((M) < 28 ? w2 : wo) + (unsigned)B2_ROW(M) * 1024u, wb, ((M) & 15) * 8192)

  const int q = (l & 15) >> 2, p = l & 3;
  const unsigned base0_0 = sm0 + (g >> 1) * 1024 + (4 * (g & 1) + q) * 64 + 32 * (0 ^ (g & 1)) + 8 * p;
  const unsigned base0_1 = base0_0 ^ 32u;
  unsigned bB0 = base0_0 + cg * 512, bB1 = base0_1 + cg * 512;
  unsigned bCS = ((rg & 1) ? bB1 : bB0) + (rg >> 1) * 4096;
  unsigned bA[2][2];
#pragma unroll
  for (int i = 0; i < 2; ++i)
#pragma unroll
    for (int ks = 0; ks < 2; ++ks)
      bA[i][ks] = sm0 + (rg >> 1) * 8192 + (32 * (rg & 1) + 16 * (i ^ cg) + r) * 128 + (((4 * ks + g) ^ (r >> 1)) << 4);

#define OUT_WT 1
  constexpr unsigned L_CNT = L_MISC + 6656;
  if (w == 5) *(LAS unsigned*)(sm0 + L_CNT + (l & 7) * 4) = 0u;
  constexpr unsigned L_RED2 = 65536;
  const f4 fz = {0.f, 0.f, 0.f, 0.f};
  const s8 ones = {0x3F80, 0x3F80, 0x3F80, 0x3F80, 0x3F80, 0x3F80, 0x3F80, 0x3F80};
  s8 A[2][16];
  s8 Bq[2][5];
  f4 acc[2][4] = {{fz, fz, fz, fz}, {fz, fz, fz, fz}};
  f4 cs = fz;

  WAIT_VM(9);
  PIN(ep); PIN(xs);
  const u64 tag = (((u64)__builtin_amdgcn_readfirstlane(ep[1]) << 32) | (unsigned)__builtin_amdgcn_readfirstlane(ep[0])) + 1ull;
  const unsigned tag32 = (unsigned)tag;
  {
    float v[8] = {bflo(xs[0]), bfhi(xs[0]), bflo(xs[1]), bfhi(xs[1]), bflo(xs[2]), bfhi(xs[2]), bflo(xs[3]), bfhi(xs[3])};
    const float m = row_sum64(((v[0] + v[1]) + (v[2] + v[3])) + ((v[4] + v[5]) + (v[6] + v[7]))) * (1.0f / 512.0f);
    float sq = 0.f;
#pragma unroll
    for (int i = 0; i < 8; ++i) { const float d = v[i] - m; sq = __builtin_fmaf(d, d, sq); }
    const float rs = __builtin_amdgcn_rsqf(row_sum64(sq) * (1.0f / 512.0f) + 1e-5f);
    u4 gr = {__float_as_uint(rs), tag32, __float_as_uint(-m * rs), tag32};
    const unsigned go = WS_ST + srow * 16;
    asm volatile("s_mov_b64 exec, 1\n\tglobal_store_dwordx4 %0, %1, %2 sc1\n\ts_mov_b64 exec, -1\n\ts_nop 1" ::"v"(go), "v"(gr), "s"(ws) : "memory");
  }
  WAIT_BAR(5);
  if (w == 0) st_flag(ws, WS_SEEN + b * 8, tag);
  PIN(pf);
#define RA(K) do { const unsigned sx_ = ((4 * ((K) >> 1)) & 7) * 8192; A[0][K] = ldsr8(bA[0][(K) & 1] + sx_); A[1][K] = ldsr8(bA[1][(K) & 1] + sx_); } while (0)
#define RB(K, T) do { const unsigned sb_ = ((4 * ((K) >> 1) + 2 + ((K) & 1)) & 7) * 8192;                              \
    const unsigned a_ = ((T) == 4 ? bCS : ((T) & 1) ? bB1 : bB0) + sb_ + ((T) == 2 || (T) == 3 ? 4096 : 0);            \
    Bq[(K) & 1][T] = trr2(a_, a_ + 2048); } while (0)
  RA(0); RB(0, 0); RB(0, 1); RB(0, 2); RB(0, 3); RB(0, 4);
  u4 st0, st1;
  const unsigned sto = WS_ST + (RG * 128 + rg * 32 + cg * 16 + r) * 16;
#pragma unroll
  for (int k = 0; k < 16; ++k) {
    const int c = k & 1;
    const bool bar = (k & 1) && k < 15;
    const int jf = (k - 1) >> 1;
    if (bar) {
      WAIT_BAR(k == 1 ? 5 : 8);
      if (jf & 1) { TOG(bB0); TOG(bB1); TOG(bCS); TOG(bA[0][0]); TOG(bA[0][1]); TOG(bA[1][0]); TOG(bA[1][1]); }
    }
#define P1F(I) do { if (jf <= 3) FILL_P1(jf + 4, I); else FILL_X(32 + 4 * (jf - 4) + (I)); } while (0)
#define P1FILL(I) do { if (k < 3) FILL_P1(k + 2, I); else if (k <= 14 && ((I) & 1) == 0) P1F(2 * (c ^ 1) + ((I) >> 1)); } while (0)
    acc[0][0] = mfma32(Bq[c][0], A[0][k], acc[0][0]); acc[1][0] = mfma32(Bq[c][0], A[1][k], acc[1][0]);
    if (k < 15) { RA(k + 1); RB(k + 1, 0); }
    SB();
    P1FILL(0);
    acc[0][1] = mfma32(Bq[c][1], A[0][k], acc[0][1]); acc[1][1] = mfma32(Bq[c][1], A[1][k], acc[1][1]);
    if (k < 15) { RB(k + 1, 1); RB(k + 1, 2); }
    SB();
    P1FILL(1);
    acc[0][2] = mfma32(Bq[c][2], A[0][k], acc[0][2]); acc[1][2] = mfma32(Bq[c][2], A[1][k], acc[1][2]);
    if (k < 15) { RB(k + 1, 3); RB(k + 1, 4); }
    SB();
    P1FILL(2);
    acc[0][3] = mfma32(Bq[c][3], A[0][k], acc[0][3]); acc[1][3] = mfma32(Bq[c][3], A[1][k], acc[1][3]);
    SB();
    P1FILL(3);
    if (k == 12) { GLD4_SC1(st0, sto, ws); const unsigned s1_ = sto ^ 256u; GLD4_SC1(st1, s1_, ws); }
    cs = mfma32(Bq[c][4], ones, cs);
    SB();
  }
  *(LAS f4*)(sm0 + L_CS1 + ((4 * (rg >> 1) + 2 * cg + (rg & 1)) * 16 + 4 * g) * 4) = cs;
  TOG(bB0); TOG(bB1); TOG(bCS);
  WAIT_BAR(4);
  PIN(st0); PIN(st1);
  if (!__all(st0[1] == tag32 && st0[3] == tag32 && st1[1] == tag32 && st1[3] == tag32)) {
#pragma nounroll
    for (unsigned spin = 0; spin < SPIN_CAP; ++spin) {
      asm volatile("global_load_dwordx4 %0, %2, %4 sc1\n\tglobal_load_dwordx4 %1, %3, %4 sc1\n\ts_waitcnt vmcnt(0)"
                   : "=&v"(st0), "=&v"(st1) : "v"(sto), "v"(sto ^ 256u), "s"(ws) : "memory");
      if (__all(st0[1] == tag32 && st0[3] == tag32 && st1[1] == tag32 && st1[3] == tag32)) break;
      __builtin_amdgcn_s_sleep(1);
    }
  }
  const float rstd[2] = {__uint_as_float(st0[0]), __uint_as_float(st1[0])}, nm[2] = {__uint_as_float(st0[2]), __uint_as_float(st1[2])};

  const __amdgpu_buffer_rsrc_t wsr = __builtin_amdgcn_make_buffer_rsrc(ws, 0, WS_END, 0x00020000);
  const int rtile0 = RG * 8 + rg * 2 + cg, rtile1 = RG * 8 + rg * 2 + (cg ^ 1);
  const unsigned hf0 = WS_HF + rtile0 * 72 * 1024, hf1 = WS_HF + rtile1 * 72 * 1024;
  const int RG2 = rt64 >> 1;
  const char* a2base = ws + WS_HF + (size_t)((rt64 * 4 + rg) * 72) * 1024;
  const char* a2c = ws + WS_HF + (size_t)((rt64 * 4 + 2 * (w & 1)) * 72 + ((w >> 1) & 1)) * 1024;
#define LOAD_A2(D, M, I) GLD4_SC1(D, l16, a2c + (unsigned)(A2_KS(M) + 72 * (I)) * 1024u)
#define TOUCH_A2(M) asm volatile("s_add_u32 m0, %2, %3\n\ts_nop 0\n\tglobal_load_lds_dwordx4 %0, %1 sc1" ::"v"(l16), "s"(a2base + (unsigned)(A2_KS(M) + cg) * 1024u), \
                                 "s"(wb), "i"(L_XR) : "memory", "scc")
  const unsigned pofs = RG2 * 128 + (l & 7) * 8;
  f4 aq0, aq1, aq2, aq3;
  s8 wp0, wp1;
  int typeB = typeA ^ 1;
  asm volatile("" : "+s"(typeB));
  if (typeA) {
    const int pf_ = odd + 2 * cg, ph_ = odd + 1;
    const unsigned oF = (pf_ >> 1) * 4096 + (pf_ & 1) * 512, oH = (ph_ >> 1) * 4096 + (ph_ & 1) * 512;
    unsigned bT[5] = {base0_0 + oF, base0_1 + oF, base0_0 + oH, base0_1 + oH, ((rg & 1) ? base0_1 : base0_0) + ((rg >> 1) ? oH : oF)};
#define RT(K, T) do { const unsigned a_ = bT[T] + ((K) & 7) * 8192; Bq[(K) & 1][T] = trr2(a_, a_ + 2048); } while (0)
#pragma unroll
    for (int lm = 0; lm < 2; ++lm) {
      const unsigned ti = 4 * lm + 2 * cg;
      f4 c0 = ldsrf4(sm0 + L_CS1 + (ti * 16 + 4 * g) * 4), c1 = ldsrf4(sm0 + L_CS1 + ((ti + 1) * 16 + 4 * g) * 4);
      u2 bb0 = *(LAS u2*)(sm0 + L_B1 + (ti * 16 + 4 * g) * 2), bb1 = *(LAS u2*)(sm0 + L_B1 + ((ti + 1) * 16 + 4 * g) * 2);
      UNPK(bf0, bb0); UNPK(bf1, bb1);
#pragma unroll
      for (int i = 0; i < 2; ++i) {
        u2 lo = h_tile(acc[i][2 * lm], c0, bf0, rstd[i], nm[i]);
        u2 hi = h_tile(acc[i][2 * lm + 1], c1, bf1, rstd[i], nm[i]);
        u4 v = {lo[0], lo[1], hi[0], hi[1]};
        __builtin_amdgcn_raw_buffer_store_b128(v, wsr, l16 + (i ? hf1 : hf0) + (4 * slice + 2 * lm + cg) * 1024, 0, 16);
      }
    }
    RT(0, 0); RT(0, 1); RT(0, 2); RT(0, 3); RT(0, 4);
    SB();
    f4 ap0 = fz, ap1 = fz, ap2 = fz, ap3 = fz, ap4 = fz, ap5 = fz;
    cs = fz;
    u2 pva;
#pragma unroll
    for (int k = 0; k < 16; ++k) {
      const int c = k & 1;
      if (k == 5) {
        WAIT_BAR(4);
        if (w == 0) st_flag(ws, WS_F1 + RG * 128 + slice * 8, tag);
      }
      if (k == 7) { TOG(bT[0]); TOG(bT[1]); TOG(bT[2]); TOG(bT[3]); TOG(bT[4]); }
      if (k == 11) WAIT_BAR(6);
      if (k == 12) { UNDEF(pva); POLL_ISSUE(pva, WS_F1 + pofs); }
      ap0 = mfma32(Bq[c][0], A[0][k], ap0); ap2 = mfma32(Bq[c][0], A[1][k], ap2);
      if (k < 15) { RT(k + 1, 0); RT(k + 1, 1); }
      SB();
      if (k < 4) FILL_X(44 + k);
      if (k >= 5 && k < 11) FILL_B2(k - 5);
      if (k >= 11 && k < 14) FILL_B2(6 + 2 * (k - 11));
      if (k == 15) { WAIT_VM(4); POLL_CHECK(pva, WS_F1 + pofs); TOUCH_A2(0); TOUCH_A2(1); }
      ap1 = mfma32(Bq[c][1], A[0][k], ap1); ap3 = mfma32(Bq[c][1], A[1][k], ap3);
      if (k < 15) { RT(k + 1, 2); RT(k + 1, 3); }
      SB();
      if (k >= 11 && k < 14) FILL_B2(7 + 2 * (k - 11));
      if (k == 15) { TOUCH_A2(2); TOUCH_A2(3); }
      ap4 = mfma32(Bq[c][2], A[0][k], ap4); ap5 = mfma32(Bq[c][3], A[0][k], ap5);
      if (k < 15) { RT(k + 1, 4); }
      cs = mfma32(Bq[c][4], ones, cs);
      SB();
    }
    *(LAS f4*)(sm0 + L_CS2 + ((2 * ((rg >> 1) ? 1 : 2 * cg) + (rg & 1)) * 16 + 4 * g) * 4) = cs;
    LDS_BAR();
    if (cg) __builtin_amdgcn_s_setprio(3);
    {
      const unsigned uF = 2 * cg;
      f4 cF0 = ldsrf4(sm0 + L_CS2 + ((2 * uF) * 16 + 4 * g) * 4), cF1 = ldsrf4(sm0 + L_CS2 + ((2 * uF + 1) * 16 + 4 * g) * 4);
      f4 cH0 = ldsrf4(sm0 + L_CS2 + (2 * 16 + 4 * g) * 4), cH1 = ldsrf4(sm0 + L_CS2 + (3 * 16 + 4 * g) * 4);
      const unsigned bbase = sm0 + L_B1 + 256 + 8 * g;
      u2 bF0_ = *(LAS u2*)(bbase + (pf_ * 32) * 2), bF1_ = *(LAS u2*)(bbase + (pf_ * 32 + 16) * 2);
      u2 bH0_ = *(LAS u2*)(bbase + (ph_ * 32) * 2), bH1_ = *(LAS u2*)(bbase + (ph_ * 32 + 16) * 2);
      UNPK(fF0, bF0_); UNPK(fF1, bF1_); UNPK(fH0, bH0_); UNPK(fH1, bH1_);
      FILL_B2(12);
#pragma unroll
      for (int i = 0; i < 2; ++i) {
        u2 lo = h_tile(i ? ap2 : ap0, cF0, fF0, rstd[i], nm[i]);
        u2 hi = h_tile(i ? ap3 : ap1, cF1, fF1, rstd[i], nm[i]);
        u4 v = {lo[0], lo[1], hi[0], hi[1]};
        __builtin_amdgcn_raw_buffer_store_b128(v, wsr, l16 + (i ? hf1 : hf0) + (32 + 3 * slice + uF) * 1024, 0, 16);
        FILL_B2(13 + i);
      }
      u2 lo = h_tile(ap4, cH0, fH0, rstd[0], nm[0]);
      u2 hi = h_tile(ap5, cH1, fH1, rstd[0], nm[0]);
      u4 v = {lo[0], lo[1], hi[0], hi[1]};
      __builtin_amdgcn_raw_buffer_store_b128(v, wsr, l16 + hf0 + (32 + 3 * slice + 1) * 1024, 0, 16);
      FILL_B2(15);
    }
#pragma unroll
    for (int i = 0; i < 2; ++i) {
#pragma unroll
      for (int k = 0; k < 16; ++k) UNDEF(A[i][k]);
#pragma unroll
      for (int k = 0; k < 4; ++k) UNDEF(acc[i][k]);
    }
  }
  if (typeB) {
#pragma unroll
    for (int c = 0; c < 2; ++c) {
      const unsigned a_ = sm0 + L_WPHI + (2 * cg + c) * 512 + (8 * (g >> 1) + q) * 32 + (2 * (g & 1) + (p >> 1)) * 8;
      u4 t = __builtin_bit_cast(u4, trr2(a_, a_ + 128));
      const unsigned sg = (l & 4) ? 0x80008000u : 0u;
      t[0] ^= sg; t[1] ^= sg; t[2] ^= sg; t[3] ^= sg;
      if (c) wp1 = __builtin_bit_cast(s8, t); else wp0 = __builtin_bit_cast(s8, t);
    }
    {
      const unsigned swz = (r >> 1) & 3;
      f4 cv0 = ldsrf4(sm0 + L_CS1 + ((4 + 2 * cg) * 16 + 4 * g) * 4), cv1 = ldsrf4(sm0 + L_CS1 + ((5 + 2 * cg) * 16 + 4 * g) * 4);
      f4 ck0 = ldsrf4(sm0 + L_CS1 + ((2 * cg) * 16 + 4 * g) * 4), ck1 = ldsrf4(sm0 + L_CS1 + ((2 * cg + 1) * 16 + 4 * g) * 4);
#pragma unroll
      for (int i = 0; i < 2; ++i) {
        float v0[4], v1[4];
#pragma unroll
        for (int e = 0; e < 4; ++e) { v0[e] = __builtin_fmaf(acc[i][2][e], rstd[i], cv0[e] * nm[i]); v1[e] = __builtin_fmaf(acc[i][3][e], rstd[i], cv1[e] * nm[i]); }
        u4 vv = {pk2(v0[0], v0[1]), pk2(v0[2], v0[3]), pk2(v1[0], v1[1]), pk2(v1[2], v1[3])};
        *(LAS u4*)(sm0 + L_V + cg * 8192 + (32 * rg + 16 * (i ^ cg) + r) * 64 + ((g ^ swz) << 4)) = vv;
      }
      const unsigned prow = sm0 + L_PHI + (2 * cg) * 8192 + (32 * rg + 16 * ((g & 1) ^ cg) + r) * 64;
      const unsigned pc0 = prow + (((2 * (g >> 1)) ^ swz) << 4), pc1 = prow + (((2 * (g >> 1) + 1) ^ swz) << 4);
      Phi ph0, ph1;
      feature_map2(acc[0][0], acc[1][0], ck0, wp0, acc[0][1], acc[1][1], ck1, wp1, rstd, nm, ph0, ph1, [](int) {});
      *(LAS u4*)(pc0) = ph0.lo; *(LAS u4*)(pc1) = ph0.hi;
      *(LAS u4*)(pc0 + 8192) = ph1.lo; *(LAS u4*)(pc1 + 8192) = ph1.hi;
    }
#define RQ(U, T) do { const unsigned a_ = ((T) == 4 ? bCS : ((T) & 1) ? bB1 : bB0) + ((4 + (U)) & 7) * 8192 + ((T) == 2 || (T) == 3 ? 4096 : 0); \
    Bq[(U) & 1][T] = trr2(a_, a_ + 2048); } while (0)
    LDS_BAR();
    RQ(0, 0); RQ(0, 1); RQ(0, 2); RQ(0, 3); RQ(0, 4);
    {
      const int hd = w >> 1, fh = w & 1;
      f4 kv = fz, zz = fz;
      const unsigned r0 = 4 * g + q;
      const unsigned swz = (r0 >> 1) & 3;
      const unsigned pa = sm0 + L_PHI + hd * 8192 + r0 * 64 + (((2 * fh + (p >> 1)) ^ swz) << 4) + 8 * (p & 1);
      const unsigned va = sm0 + L_V + (hd >> 1) * 8192 + r0 * 64 + ((p ^ swz) << 4) + 8 * (hd & 1);
#pragma unroll
      for (int R = 0; R < 4; ++R) {
        s8 pf8 = trr2(pa + R * 2048, pa + R * 2048 + 1024), vf8 = trr2(va + R * 2048, va + R * 2048 + 1024);
        kv = mfma32(pf8, vf8, kv);
        zz = mfma32(pf8, ones, zz);
      }
      u2 kvb = {pk2(kv[0], kv[1]), pk2(kv[2], kv[3])};
      u2 zb = {pk2(zz[0], zz[1]), pk2(zz[2], zz[3])};
      __builtin_amdgcn_raw_buffer_store_b64(kvb, wsr, l16 + WS_KVP + ((slice * 8 + w) * 8 + (RG >> 1)) * 1024 + 8 * (RG & 1), 0, 16);
      __builtin_amdgcn_raw_buffer_store_b64(zb, wsr, r == 0 ? (unsigned)((g >> 1) * 256 + 8 * (g & 1)) + WS_ZP + (slice * 8 + w) * 512 + RG * 16 : 0x7ffffff0u, 0, 16);
    }
    SB();
    aq0 = fz; aq1 = fz; aq2 = fz; aq3 = fz;
    cs = fz;
    u2 pvb;
    const unsigned kofs = l < 16 ? WS_FK + slice * 128 + l * 8 : WS_F1 + pofs;
#pragma unroll
    for (int u = 0; u < 8; ++u) {
      const int c = u & 1;
      if (u == 3) {
        WAIT_BAR(0);
        if (w == 0) st_flag(ws, WS_FK + slice * 128 + RG * 8, tag);
        TOG(bB0); TOG(bB1); TOG(bCS);
      }
      if (u == 6) { UNDEF(pvb); POLL_ISSUE(pvb, kofs); }
      aq0 = mfma32(Bq[c][0], A[0][2 * u], aq0); aq2 = mfma32(Bq[c][0], A[1][2 * u], aq2);
      if (u < 7) { RQ(u + 1, 0); RQ(u + 1, 1); }
      SB();
      if (u >= 3 && u < 7) FILL_B2(u < 5 ? 2 * u - 2 : 2 * u + 2);
      aq1 = mfma32(Bq[c][1], A[0][2 * u], aq1); aq3 = mfma32(Bq[c][1], A[1][2 * u], aq3);
      if (u < 7) { RQ(u + 1, 2); RQ(u + 1, 3); }
      SB();
      aq0 = mfma32(Bq[c][2], A[0][2 * u + 1], aq0); aq2 = mfma32(Bq[c][2], A[1][2 * u + 1], aq2);
      if (u < 7) { RQ(u + 1, 4); }
      SB();
      if (u >= 3 && u < 7) FILL_B2(u < 5 ? 2 * u - 1 : 2 * u + 3);
      aq1 = mfma32(Bq[c][3], A[0][2 * u + 1], aq1); aq3 = mfma32(Bq[c][3], A[1][2 * u + 1], aq3);
      cs = mfma32(Bq[c][4], ones, cs);
      SB();
    }
    *(LAS f4*)(sm0 + L_CS2 + ((4 * (rg >> 1) + 2 * cg + (rg & 1)) * 16 + 4 * g) * 4) = cs;
    WAIT_BAR(2);
    POLL_CHECK(pvb, kofs);
  }
#define GS1(G) do { PIN(G); sm0_ = dot2(G[0], selLo, sm0_); sm1_ = dot2(G[0], selHi, sm1_); sm2_ = dot2(G[1], selLo, sm2_); sm3_ = dot2(G[1], selHi, sm3_); \
                    sm0_ = dot2(G[2], selLo, sm0_); sm1_ = dot2(G[2], selHi, sm1_); sm2_ = dot2(G[3], selLo, sm2_); sm3_ = dot2(G[3], selHi, sm3_); } while (0)
#define GATHER_SUM() do { float sm0_ = 0.f, sm1_ = 0.f, sm2_ = 0.f, sm3_ = 0.f; unsigned selLo = 0x00003F80u, selHi = 0x3F800000u;        \
    PIN(selLo); PIN(selHi);                                        \
    GS1(gp0); GS1(gp1); GS1(gp2); GS1(gp3); GS1(gp4); GS1(gp5); GS1(gp6); GS1(gp7); PIN(zf);                                              \
    u2 kb = {pk2(sm0_, sm1_), pk2(sm2_, sm3_)};                                                                                           \
    const unsigned ib = sm0 + L_V + (w >> 1) * 2048 + (w & 1) * 512;                                                                      \
    *(LAS u2*)(ib + (g >> 1) * 1024 + r * 16 + 8 * (g & 1)) = kb;                                                                         \
    *(LAS u4*)(ib + ((l >> 4) & 1) * 1024 + 256 + r * 16) = zf; } while (0)
#define O_STEP() do { u4 o0, o1;                                                                                                          \
    _Pragma("unroll") for (int cc = 0; cc < 2; ++cc) {                                                                                    \
      s8 k0 = ldsr8(sm0 + L_V + (2 * cg + cc) * 2048 + l16), k1 = ldsr8(sm0 + L_V + (2 * cg + cc) * 2048 + 1024 + l16);                   \
      f16v z; _Pragma("unroll") for (int i = 0; i < 16; ++i) z[i] = 0.f;                                                                  \
      z = mfmaW(k0, __builtin_bit_cast(s8, cc ? phq1.lo : phq0.lo), z);                                                                   \
      z = mfmaW(k1, __builtin_bit_cast(s8, cc ? phq1.hi : phq0.hi), z);                                                                   \
      float den = xsum32(((z[8] + z[9]) + (z[10] + z[11])) + ((z[12] + z[13]) + (z[14] + z[15]))) + 1e-6f;                                \
      float id = __builtin_amdgcn_rcpf(den);                                                                                              \
      o0[2 * cc] = pk2(z[0] * id, z[1] * id); o0[2 * cc + 1] = pk2(z[2] * id, z[3] * id);                                                 \
      o1[2 * cc] = pk2(z[4] * id, z[5] * id); o1[2 * cc + 1] = pk2(z[6] * id, z[7] * id); }                                               \
                                                                     \
    const unsigned oo = ((g & 1) ? hf1 : hf0) + (r + 16 * (g >> 1)) * 16 + (56 + 2 * slice + cg) * 1024;                                  \
    __builtin_amdgcn_raw_buffer_store_b128(o0, wsr, oo, 0, 16);                                                                           \
    __builtin_amdgcn_raw_buffer_store_b128(o1, wsr, oo + 512, 0, 16); } while (0)
  if (typeB) {
    Phi phq0, phq1;
    u4 gp0, gp1, gp2, gp3, gp4, gp5, gp6, gp7, zf;
    const char* gb = ws + WS_KVP + (size_t)(slice * 8 + w) * 8192;
    const char* zb_ = ws + WS_ZP + (size_t)(slice * 8 + w) * 512;
    const unsigned zo = l16 & 511u;
    f4 ca0 = ldsrf4(sm0 + L_CS2 + ((2 * cg) * 16 + 4 * g) * 4), cb0 = ldsrf4(sm0 + L_CS2 + ((4 + 2 * cg) * 16 + 4 * g) * 4);
    f4 ca1 = ldsrf4(sm0 + L_CS2 + ((2 * cg + 1) * 16 + 4 * g) * 4), cb1 = ldsrf4(sm0 + L_CS2 + ((5 + 2 * cg) * 16 + 4 * g) * 4);
    TOUCH_A2(0); TOUCH_A2(1);
    GLD4_SC1(gp0, l16, gb); GLD4_SC1(gp1, l16, gb + 1024); GLD4_SC1(gp2, l16, gb + 2048);
    feature_map2(aq0, aq2, ca0 + cb0, wp0, aq1, aq3, ca1 + cb1, wp1, rstd, nm, phq0, phq1, [&](int st) __attribute__((always_inline)) {
      if (st == 0) { GLD4_SC1(gp3, l16, gb + 3072); GLD4_SC1(gp4, l16, gb + 4096); GLD4_SC1(gp5, l16, gb + 5120); }
      if (st == 1) { GLD4_SC1(gp6, l16, gb + 6144); GLD4_SC1(gp7, l16, gb + 7168); GLD4_SC1(zf, zo, zb_); }
      if (st == 2) { FILL_B2(8); FILL_B2(9); }
      if (st == 3) { FILL_B2(10); FILL_B2(11); }
    });
    PIN(phq0.lo); PIN(phq0.hi); PIN(phq1.lo); PIN(phq1.hi);
    SB();
    WAIT_VM(4);
    GATHER_SUM();
    LDS_BAR();
    if (cg) __builtin_amdgcn_s_setprio(3);
    O_STEP();
  }

  const char* xt = x + (size_t)rt64 * 65536 + ct * 128;
  unsigned lz = l16; PIN(lz);
  const unsigned voffX2 = (8 * w + (lz >> 7)) * 1024 + ((((lz >> 4) & 7) ^ ((4 * (w & 1) + (lz >> 8)) & 7)) << 4);
#define ARRIVE2() do { unsigned old_; const unsigned ca_ = sm0 + L_CNT, one_ = 1u;                                          \
    asm volatile("s_mov_b64 exec, 1\n\tds_add_rtn_u32 %0, %1, %2\n\ts_mov_b64 exec, -1\n\ts_waitcnt lgkmcnt(0)" : "=&v"(old_) : "v"(ca_), "v"(one_) : "memory"); \
    if (__builtin_amdgcn_readfirstlane(old_) == 7u) st_flag(ws, (typeA ? WS_F2 : WS_FO) + RG * 128 + slice * 8, tag); } while (0)
#define LDS_SPIN(OFF) do { _Pragma("nounroll") for (unsigned spin = 0; spin < (SPIN_CAP << 4); ++spin) {                    \
    if (__builtin_amdgcn_readfirstlane(*(volatile LAS unsigned*)(sm0 + (OFF))) != 0u) break;                                \
    __builtin_amdgcn_s_sleep(1); } } while (0)
#define EPOCH_BUMP() if (b == 0 && w == 5) { const unsigned so = WS_SEEN + l * 32, tl = (unsigned)tag, th = (unsigned)(tag >> 32); \
    _Pragma("nounroll") for (unsigned spin = 0; spin < SPIN_CAP; ++spin) { u4 s0_, s1_;                                     \
      asm volatile("global_load_dwordx4 %0, %2, %3 sc1\n\tglobal_load_dwordx4 %1, %2, %3 offset:16 sc1\n\ts_waitcnt vmcnt(0)" : "=&v"(s0_), "=&v"(s1_) : "v"(so), "s"(ws) : "memory"); \
      if (__all(s0_[0] == tl && s0_[1] == th && s0_[2] == tl && s0_[3] == th && s1_[0] == tl && s1_[1] == th && s1_[2] == tl && s1_[3] == th)) break; \
      __builtin_amdgcn_s_sleep(1); }                                                                                        \
    st_flag(ws, WS_E, tag); }
#define FILL2_B2(M) do { const char* s_ = ((M) < 28 ? w2 : wo) + (unsigned)B2_ROW(M) * 1024u;                               \
    GLDS(voffB2, s_ - 32768, wb, ((M) & 15) * 8192 - 4096); GLDS(voffB2, s_, wb, ((M) & 15) * 8192); } while (0)
  const unsigned sbuf = sm0 + L_RED + (2 * (w & 1)) * 8192 + ((w >> 1) & 1) * 4096 + l16;
#define SEND(A) do { *(LAS f4*)(sbuf) = A##0; *(LAS f4*)(sbuf + 1024) = A##1; *(LAS f4*)(sbuf + 2048) = A##2; *(LAS f4*)(sbuf + 3072) = A##3;       \
    *(LAS f4*)(sbuf + 8192) = A##4; *(LAS f4*)(sbuf + 9216) = A##5; *(LAS f4*)(sbuf + 10240) = A##6; *(LAS f4*)(sbuf + 11264) = A##7; } while (0)
  const unsigned rbuf = sm0 + L_RED + rg * 8192 + l16;
#define GATE_CALC() _Pragma("unroll") for (int t = 0; t < 4; ++t) { f4 m_ = ldsrf4(rbuf + t * 1024) + ldsrf4(rbuf + 4096 + t * 1024); \
    u2 bb_ = *(LAS u2*)(sm0 + L_B2 + (16 * t + 4 * g) * 2); UNPK(bv_, bb_);                                                 \
    _Pragma("unroll") for (int e = 0; e < 4; ++e) { float t_ = m_[e] + bv_[e];                                              \
      gt[t][e] = t_ * __builtin_amdgcn_rcpf(1.0f + __builtin_amdgcn_exp2f(-1.4426950408889634f * t_)); } }
  int isld = cg;
  asm volatile("" : "+s"(isld));
  if (cg == 0) {
    unsigned b2r0 = base0_0 + ((w >> 1) & 1) * 4096, b2r1 = base0_1 + ((w >> 1) & 1) * 4096;
    u4 A2_0_0, A2_0_1, A2_1_0, A2_1_1, A2_2_0, A2_2_1, A2_3_0, A2_3_1, A2_4_0, A2_4_1, A2_5_0, A2_5_1, A2_6_0, A2_6_1, A2_7_0, A2_7_1, A2_8_0, A2_8_1, A2_9_0, A2_9_1, A2_10_0, A2_10_1, A2_11_0, A2_11_1, A2_12_0, A2_12_1, A2_13_0, A2_13_1, A2_14_0, A2_14_1, A2_15_0, A2_15_1, A2_16_0, A2_16_1, A2_17_0, A2_17_1, A2_18_0, A2_18_1, A2_19_0, A2_19_1, A2_20_0, A2_20_1, A2_21_0, A2_21_1, A2_22_0, A2_22_1, A2_23_0, A2_23_1, A2_24_0, A2_24_1, A2_25_0, A2_25_1, A2_26_0, A2_26_1, A2_27_0, A2_27_1, A2_28_0, A2_28_1, A2_29_0, A2_29_1, A2_30_0, A2_30_1, A2_31_0, A2_31_1, A2_32_0, A2_32_1, A2_33_0, A2_33_1, A2_34_0, A2_34_1, A2_35_0, A2_35_1;
    s8 W0_0, W0_1, W0_2, W0_3, W1_0, W1_1, W1_2, W1_3;

    f4 aM0 = fz, aM1 = fz, aM2 = fz, aM3 = fz, aM4 = fz, aM5 = fz, aM6 = fz, aM7 = fz;
    f4 aS0 = fz, aS1 = fz, aS2 = fz, aS3 = fz, aS4 = fz, aS5 = fz, aS6 = fz, aS7 = fz;
    LOAD_A2(A2_0_0, 0, 0);
    LOAD_A2(A2_0_1, 0, 1);
    LOAD_A2(A2_1_0, 1, 0);
    LOAD_A2(A2_1_1, 1, 1);
    LOAD_A2(A2_2_0, 2, 0);
    LOAD_A2(A2_2_1, 2, 1);
    LOAD_A2(A2_3_0, 3, 0);
    LOAD_A2(A2_3_1, 3, 1);
    LOAD_A2(A2_4_0, 4, 0);
    LOAD_A2(A2_4_1, 4, 1);
    LOAD_A2(A2_5_0, 5, 0);
    LOAD_A2(A2_5_1, 5, 1);
    LOAD_A2(A2_6_0, 6, 0);
    LOAD_A2(A2_6_1, 6, 1);
    LOAD_A2(A2_7_0, 7, 0);
    LOAD_A2(A2_7_1, 7, 1);
    W0_0 = trr2(b2r0 + 0, b2r0 + 2048); W0_1 = trr2(b2r1 + 0, b2r1 + 2048); W0_2 = trr2(b2r0 + 512, b2r0 + 2560); W0_3 = trr2(b2r1 + 512, b2r1 + 2560);
    SB();
    WAIT_VM(14);
    ARRIVE2();
    PIN(A2_0_0); PIN(A2_0_1);
    aM0 = mfma32(W0_0, __builtin_bit_cast(s8, A2_0_0), aM0); W1_0 = trr2(b2r0 + 8192, b2r0 + 10240); SB();
    aM4 = mfma32(W0_0, __builtin_bit_cast(s8, A2_0_1), aM4); W1_1 = trr2(b2r1 + 8192, b2r1 + 10240); SB();
    aM1 = mfma32(W0_1, __builtin_bit_cast(s8, A2_0_0), aM1); W1_2 = trr2(b2r0 + 8704, b2r0 + 10752); SB();
    aM5 = mfma32(W0_1, __builtin_bit_cast(s8, A2_0_1), aM5); W1_3 = trr2(b2r1 + 8704, b2r1 + 10752); SB();
    LOAD_A2(A2_8_0, 8, 0);
    aM2 = mfma32(W0_2, __builtin_bit_cast(s8, A2_0_0), aM2);  SB();
    LOAD_A2(A2_8_1, 8, 1);
    aM6 = mfma32(W0_2, __builtin_bit_cast(s8, A2_0_1), aM6);  SB();
    aM3 = mfma32(W0_3, __builtin_bit_cast(s8, A2_0_0), aM3);  SB();
    aM7 = mfma32(W0_3, __builtin_bit_cast(s8, A2_0_1), aM7);  SB();
    WAIT_VM(14);
    PIN(A2_1_0); PIN(A2_1_1);
    PIN(aM0); aM0 = mfma32(W1_0, __builtin_bit_cast(s8, A2_1_0), aM0); W0_0 = trr2(b2r0 + 16384, b2r0 + 18432); SB();
    PIN(aM4); aM4 = mfma32(W1_0, __builtin_bit_cast(s8, A2_1_1), aM4); W0_1 = trr2(b2r1 + 16384, b2r1 + 18432); SB();
    PIN(aM1); aM1 = mfma32(W1_1, __builtin_bit_cast(s8, A2_1_0), aM1); W0_2 = trr2(b2r0 + 16896, b2r0 + 18944); SB();
    PIN(aM5); aM5 = mfma32(W1_1, __builtin_bit_cast(s8, A2_1_1), aM5); W0_3 = trr2(b2r1 + 16896, b2r1 + 18944); SB();
    LOAD_A2(A2_9_0, 9, 0);
    PIN(aM2); aM2 = mfma32(W1_2, __builtin_bit_cast(s8, A2_1_0), aM2);  SB();
    LOAD_A2(A2_9_1, 9, 1);
    PIN(aM6); aM6 = mfma32(W1_2, __builtin_bit_cast(s8, A2_1_1), aM6);  SB();
    PIN(aM3); aM3 = mfma32(W1_3, __builtin_bit_cast(s8, A2_1_0), aM3);  SB();
    PIN(aM7); aM7 = mfma32(W1_3, __builtin_bit_cast(s8, A2_1_1), aM7);  SB();
    WAIT_VM(14);
    PIN(A2_2_0); PIN(A2_2_1);
    PIN(aM0); aM0 = mfma32(W0_0, __builtin_bit_cast(s8, A2_2_0), aM0); W1_0 = trr2(b2r0 + 24576, b2r0 + 26624); SB();
    PIN(aM4); aM4 = mfma32(W0_0, __builtin_bit_cast(s8, A2_2_1), aM4); W1_1 = trr2(b2r1 + 24576, b2r1 + 26624); SB();
    PIN(aM1); aM1 = mfma32(W0_1, __builtin_bit_cast(s8, A2_2_0), aM1); W1_2 = trr2(b2r0 + 25088, b2r0 + 27136); SB();
    PIN(aM5); aM5 = mfma32(W0_1, __builtin_bit_cast(s8, A2_2_1), aM5); W1_3 = trr2(b2r1 + 25088, b2r1 + 27136); SB();
    LOAD_A2(A2_10_0, 10, 0);
    PIN(aM2); aM2 = mfma32(W0_2, __builtin_bit_cast(s8, A2_2_0), aM2);  SB();
    LOAD_A2(A2_10_1, 10, 1);
    PIN(aM6); aM6 = mfma32(W0_2, __builtin_bit_cast(s8, A2_2_1), aM6);  SB();
    PIN(aM3); aM3 = mfma32(W0_3, __builtin_bit_cast(s8, A2_2_0), aM3);  SB();
    PIN(aM7); aM7 = mfma32(W0_3, __builtin_bit_cast(s8, A2_2_1), aM7);  SB();
    WAIT_BAR(14);
    PIN(A2_3_0); PIN(A2_3_1);
    PIN(aM0); aM0 = mfma32(W1_0, __builtin_bit_cast(s8, A2_3_0), aM0); W0_0 = trr2(b2r0 + 32768, b2r0 + 34816); SB();
    PIN(aM4); aM4 = mfma32(W1_0, __builtin_bit_cast(s8, A2_3_1), aM4); W0_1 = trr2(b2r1 + 32768, b2r1 + 34816); SB();
    PIN(aM1); aM1 = mfma32(W1_1, __builtin_bit_cast(s8, A2_3_0), aM1); W0_2 = trr2(b2r0 + 33280, b2r0 + 35328); SB();
    PIN(aM5); aM5 = mfma32(W1_1, __builtin_bit_cast(s8, A2_3_1), aM5); W0_3 = trr2(b2r1 + 33280, b2r1 + 35328); SB();
    LOAD_A2(A2_11_0, 11, 0);
    PIN(aM2); aM2 = mfma32(W1_2, __builtin_bit_cast(s8, A2_3_0), aM2);  SB();
    LOAD_A2(A2_11_1, 11, 1);
    PIN(aM6); aM6 = mfma32(W1_2, __builtin_bit_cast(s8, A2_3_1), aM6);  SB();
    PIN(aM3); aM3 = mfma32(W1_3, __builtin_bit_cast(s8, A2_3_0), aM3);  SB();
    PIN(aM7); aM7 = mfma32(W1_3, __builtin_bit_cast(s8, A2_3_1), aM7);  SB();
    WAIT_VM(14);
    PIN(A2_4_0); PIN(A2_4_1);
    PIN(aM0); aM0 = mfma32(W0_0, __builtin_bit_cast(s8, A2_4_0), aM0); W1_0 = trr2(b2r0 + 40960, b2r0 + 43008); SB();
    PIN(aM4); aM4 = mfma32(W0_0, __builtin_bit_cast(s8, A2_4_1), aM4); W1_1 = trr2(b2r1 + 40960, b2r1 + 43008); SB();
    PIN(aM1); aM1 = mfma32(W0_1, __builtin_bit_cast(s8, A2_4_0), aM1); W1_2 = trr2(b2r0 + 41472, b2r0 + 43520); SB();
    PIN(aM5); aM5 = mfma32(W0_1, __builtin_bit_cast(s8, A2_4_1), aM5); W1_3 = trr2(b2r1 + 41472, b2r1 + 43520); SB();
    LOAD_A2(A2_12_0, 12, 0);
    PIN(aM2); aM2 = mfma32(W0_2, __builtin_bit_cast(s8, A2_4_0), aM2);  SB();
    LOAD_A2(A2_12_1, 12, 1);
    PIN(aM6); aM6 = mfma32(W0_2, __builtin_bit_cast(s8, A2_4_1), aM6);  SB();
    PIN(aM3); aM3 = mfma32(W0_3, __builtin_bit_cast(s8, A2_4_0), aM3);  SB();
    PIN(aM7); aM7 = mfma32(W0_3, __builtin_bit_cast(s8, A2_4_1), aM7);  SB();
    WAIT_VM(14);
    PIN(A2_5_0); PIN(A2_5_1);
    PIN(aM0); aM0 = mfma32(W1_0, __builtin_bit_cast(s8, A2_5_0), aM0); W0_0 = trr2(b2r0 + 49152, b2r0 + 51200); SB();
    PIN(aM4); aM4 = mfma32(W1_0, __builtin_bit_cast(s8, A2_5_1), aM4); W0_1 = trr2(b2r1 + 49152, b2r1 + 51200); SB();
    PIN(aM1); aM1 = mfma32(W1_1, __builtin_bit_cast(s8, A2_5_0), aM1); W0_2 = trr2(b2r0 + 49664, b2r0 + 51712); SB();
    PIN(aM5); aM5 = mfma32(W1_1, __builtin_bit_cast(s8, A2_5_1), aM5); W0_3 = trr2(b2r1 + 49664, b2r1 + 51712); SB();
    LOAD_A2(A2_13_0, 13, 0);
    PIN(aM2); aM2 = mfma32(W1_2, __builtin_bit_cast(s8, A2_5_0), aM2);  SB();
    LOAD_A2(A2_13_1, 13, 1);
    PIN(aM6); aM6 = mfma32(W1_2, __builtin_bit_cast(s8, A2_5_1), aM6);  SB();
    PIN(aM3); aM3 = mfma32(W1_3, __builtin_bit_cast(s8, A2_5_0), aM3);  SB();
    PIN(aM7); aM7 = mfma32(W1_3, __builtin_bit_cast(s8, A2_5_1), aM7);  SB();
    WAIT_VM(14);
    PIN(A2_6_0); PIN(A2_6_1);
    PIN(aM0); aM0 = mfma32(W0_0, __builtin_bit_cast(s8, A2_6_0), aM0); W1_0 = trr2(b2r0 + 57344, b2r0 + 59392); SB();
    PIN(aM4); aM4 = mfma32(W0_0, __builtin_bit_cast(s8, A2_6_1), aM4); W1_1 = trr2(b2r1 + 57344, b2r1 + 59392); SB();
    PIN(aM1); aM1 = mfma32(W0_1, __builtin_bit_cast(s8, A2_6_0), aM1); W1_2 = trr2(b2r0 + 57856, b2r0 + 59904); SB();
    PIN(aM5); aM5 = mfma32(W0_1, __builtin_bit_cast(s8, A2_6_1), aM5); W1_3 = trr2(b2r1 + 57856, b2r1 + 59904); SB();
    LOAD_A2(A2_14_0, 14, 0);
    PIN(aM2); aM2 = mfma32(W0_2, __builtin_bit_cast(s8, A2_6_0), aM2);  SB();
    LOAD_A2(A2_14_1, 14, 1);
    PIN(aM6); aM6 = mfma32(W0_2, __builtin_bit_cast(s8, A2_6_1), aM6);  SB();
    PIN(aM3); aM3 = mfma32(W0_3, __builtin_bit_cast(s8, A2_6_0), aM3);  SB();
    PIN(aM7); aM7 = mfma32(W0_3, __builtin_bit_cast(s8, A2_6_1), aM7);  SB();
    WAIT_BAR(14);
    PIN(A2_7_0); PIN(A2_7_1);
    TOG(b2r0); TOG(b2r1);
    PIN(aM0); aM0 = mfma32(W1_0, __builtin_bit_cast(s8, A2_7_0), aM0); W0_0 = trr2(b2r0 + 0, b2r0 + 2048); SB();
    PIN(aM4); aM4 = mfma32(W1_0, __builtin_bit_cast(s8, A2_7_1), aM4); W0_1 = trr2(b2r1 + 0, b2r1 + 2048); SB();
    PIN(aM1); aM1 = mfma32(W1_1, __builtin_bit_cast(s8, A2_7_0), aM1); W0_2 = trr2(b2r0 + 512, b2r0 + 2560); SB();
    PIN(aM5); aM5 = mfma32(W1_1, __builtin_bit_cast(s8, A2_7_1), aM5); W0_3 = trr2(b2r1 + 512, b2r1 + 2560); SB();
    LOAD_A2(A2_15_0, 15, 0);
    PIN(aM2); aM2 = mfma32(W1_2, __builtin_bit_cast(s8, A2_7_0), aM2);  SB();
    LOAD_A2(A2_15_1, 15, 1);
    PIN(aM6); aM6 = mfma32(W1_2, __builtin_bit_cast(s8, A2_7_1), aM6);  SB();
    PIN(aM3); aM3 = mfma32(W1_3, __builtin_bit_cast(s8, A2_7_0), aM3);  SB();
    PIN(aM7); aM7 = mfma32(W1_3, __builtin_bit_cast(s8, A2_7_1), aM7);  SB();
    WAIT_VM(14);
    PIN(A2_8_0); PIN(A2_8_1);
    PIN(aM0); aM0 = mfma32(W0_0, __builtin_bit_cast(s8, A2_8_0), aM0); W1_0 = trr2(b2r0 + 8192, b2r0 + 10240); SB();
    PIN(aM4); aM4 = mfma32(W0_0, __builtin_bit_cast(s8, A2_8_1), aM4); W1_1 = trr2(b2r1 + 8192, b2r1 + 10240); SB();
    PIN(aM1); aM1 = mfma32(W0_1, __builtin_bit_cast(s8, A2_8_0), aM1); W1_2 = trr2(b2r0 + 8704, b2r0 + 10752); SB();
    PIN(aM5); aM5 = mfma32(W0_1, __builtin_bit_cast(s8, A2_8_1), aM5); W1_3 = trr2(b2r1 + 8704, b2r1 + 10752); SB();
    PIN(aM2); aM2 = mfma32(W0_2, __builtin_bit_cast(s8, A2_8_0), aM2);  SB();
    PIN(aM6); aM6 = mfma32(W0_2, __builtin_bit_cast(s8, A2_8_1), aM6);  SB();
    PIN(aM3); aM3 = mfma32(W0_3, __builtin_bit_cast(s8, A2_8_0), aM3);  SB();
    PIN(aM7); aM7 = mfma32(W0_3, __builtin_bit_cast(s8, A2_8_1), aM7);  SB();
    WAIT_VM(12);
    PIN(A2_9_0); PIN(A2_9_1);
    PIN(aM0); aM0 = mfma32(W1_0, __builtin_bit_cast(s8, A2_9_0), aM0); W0_0 = trr2(b2r0 + 16384, b2r0 + 18432); SB();
    PIN(aM4); aM4 = mfma32(W1_0, __builtin_bit_cast(s8, A2_9_1), aM4); W0_1 = trr2(b2r1 + 16384, b2r1 + 18432); SB();
    PIN(aM1); aM1 = mfma32(W1_1, __builtin_bit_cast(s8, A2_9_0), aM1); W0_2 = trr2(b2r0 + 16896, b2r0 + 18944); SB();
    PIN(aM5); aM5 = mfma32(W1_1, __builtin_bit_cast(s8, A2_9_1), aM5); W0_3 = trr2(b2r1 + 16896, b2r1 + 18944); SB();
    PIN(aM2); aM2 = mfma32(W1_2, __builtin_bit_cast(s8, A2_9_0), aM2);  SB();
    PIN(aM6); aM6 = mfma32(W1_2, __builtin_bit_cast(s8, A2_9_1), aM6);  SB();
    PIN(aM3); aM3 = mfma32(W1_3, __builtin_bit_cast(s8, A2_9_0), aM3);  SB();
    PIN(aM7); aM7 = mfma32(W1_3, __builtin_bit_cast(s8, A2_9_1), aM7);  SB();
    WAIT_VM(10);
    LDS_SPIN(L_CNT + 16);
    PIN(A2_10_0); PIN(A2_10_1);
    PIN(aM0); aM0 = mfma32(W0_0, __builtin_bit_cast(s8, A2_10_0), aM0); W1_0 = trr2(b2r0 + 24576, b2r0 + 26624); SB();
    PIN(aM4); aM4 = mfma32(W0_0, __builtin_bit_cast(s8, A2_10_1), aM4); W1_1 = trr2(b2r1 + 24576, b2r1 + 26624); SB();
    PIN(aM1); aM1 = mfma32(W0_1, __builtin_bit_cast(s8, A2_10_0), aM1); W1_2 = trr2(b2r0 + 25088, b2r0 + 27136); SB();
    LOAD_A2(A2_16_0, 16, 0);
    PIN(aM5); aM5 = mfma32(W0_1, __builtin_bit_cast(s8, A2_10_1), aM5); W1_3 = trr2(b2r1 + 25088, b2r1 + 27136); SB();
    LOAD_A2(A2_16_1, 16, 1);
    PIN(aM2); aM2 = mfma32(W0_2, __builtin_bit_cast(s8, A2_10_0), aM2);  SB();
    LOAD_A2(A2_17_0, 17, 0);
    PIN(aM6); aM6 = mfma32(W0_2, __builtin_bit_cast(s8, A2_10_1), aM6);  SB();
    LOAD_A2(A2_17_1, 17, 1);
    PIN(aM3); aM3 = mfma32(W0_3, __builtin_bit_cast(s8, A2_10_0), aM3);  SB();
    LOAD_A2(A2_18_0, 18, 0);
    PIN(aM7); aM7 = mfma32(W0_3, __builtin_bit_cast(s8, A2_10_1), aM7);  SB();
    LOAD_A2(A2_18_1, 18, 1);
    WAIT_BAR(14);
    PIN(A2_11_0); PIN(A2_11_1);
    PIN(aM0); aM0 = mfma32(W1_0, __builtin_bit_cast(s8, A2_11_0), aM0); W0_0 = trr2(b2r0 + 32768, b2r0 + 34816); SB();
    PIN(aM4); aM4 = mfma32(W1_0, __builtin_bit_cast(s8, A2_11_1), aM4); W0_1 = trr2(b2r1 + 32768, b2r1 + 34816); SB();
    PIN(aM1); aM1 = mfma32(W1_1, __builtin_bit_cast(s8, A2_11_0), aM1); W0_2 = trr2(b2r0 + 33280, b2r0 + 35328); SB();
    PIN(aM5); aM5 = mfma32(W1_1, __builtin_bit_cast(s8, A2_11_1), aM5); W0_3 = trr2(b2r1 + 33280, b2r1 + 35328); SB();
    LOAD_A2(A2_19_0, 19, 0);
    PIN(aM2); aM2 = mfma32(W1_2, __builtin_bit_cast(s8, A2_11_0), aM2);  SB();
    LOAD_A2(A2_19_1, 19, 1);
    PIN(aM6); aM6 = mfma32(W1_2, __builtin_bit_cast(s8, A2_11_1), aM6);  SB();
    PIN(aM3); aM3 = mfma32(W1_3, __builtin_bit_cast(s8, A2_11_0), aM3);  SB();
    PIN(aM7); aM7 = mfma32(W1_3, __builtin_bit_cast(s8, A2_11_1), aM7);  SB();
    WAIT_VM(14);
    PIN(A2_12_0); PIN(A2_12_1);
    PIN(aM0); aM0 = mfma32(W0_0, __builtin_bit_cast(s8, A2_12_0), aM0); W1_0 = trr2(b2r0 + 40960, b2r0 + 43008); SB();
    PIN(aM4); aM4 = mfma32(W0_0, __builtin_bit_cast(s8, A2_12_1), aM4); W1_1 = trr2(b2r1 + 40960, b2r1 + 43008); SB();
    PIN(aM1); aM1 = mfma32(W0_1, __builtin_bit_cast(s8, A2_12_0), aM1); W1_2 = trr2(b2r0 + 41472, b2r0 + 43520); SB();
    PIN(aM5); aM5 = mfma32(W0_1, __builtin_bit_cast(s8, A2_12_1), aM5); W1_3 = trr2(b2r1 + 41472, b2r1 + 43520); SB();
    LOAD_A2(A2_20_0, 20, 0);
    PIN(aM2); aM2 = mfma32(W0_2, __builtin_bit_cast(s8, A2_12_0), aM2);  SB();
    LOAD_A2(A2_20_1, 20, 1);
    PIN(aM6); aM6 = mfma32(W0_2, __builtin_bit_cast(s8, A2_12_1), aM6);  SB();
    PIN(aM3); aM3 = mfma32(W0_3, __builtin_bit_cast(s8, A2_12_0), aM3);  SB();
    PIN(aM7); aM7 = mfma32(W0_3, __builtin_bit_cast(s8, A2_12_1), aM7);  SB();
    WAIT_VM(14);
    PIN(A2_13_0); PIN(A2_13_1);
    PIN(aM0); aM0 = mfma32(W1_0, __builtin_bit_cast(s8, A2_13_0), aM0); W0_0 = trr2(b2r0 + 49152, b2r0 + 51200); SB();
    PIN(aM4); aM4 = mfma32(W1_0, __builtin_bit_cast(s8, A2_13_1), aM4); W0_1 = trr2(b2r1 + 49152, b2r1 + 51200); SB();
    PIN(aM1); aM1 = mfma32(W1_1, __builtin_bit_cast(s8, A2_13_0), aM1); W0_2 = trr2(b2r0 + 49664, b2r0 + 51712); SB();
    PIN(aM5); aM5 = mfma32(W1_1, __builtin_bit_cast(s8, A2_13_1), aM5); W0_3 = trr2(b2r1 + 49664, b2r1 + 51712); SB();
    LOAD_A2(A2_21_0, 21, 0);
    PIN(aM2); aM2 = mfma32(W1_2, __builtin_bit_cast(s8, A2_13_0), aM2);  SB();
    LOAD_A2(A2_21_1, 21, 1);
    PIN(aM6); aM6 = mfma32(W1_2, __builtin_bit_cast(s8, A2_13_1), aM6);  SB();
    PIN(aM3); aM3 = mfma32(W1_3, __builtin_bit_cast(s8, A2_13_0), aM3);  SB();
    PIN(aM7); aM7 = mfma32(W1_3, __builtin_bit_cast(s8, A2_13_1), aM7);  SB();
    WAIT_VM(14);
    PIN(A2_14_0); PIN(A2_14_1);
    PIN(aM0); aM0 = mfma32(W0_0, __builtin_bit_cast(s8, A2_14_0), aM0); W1_0 = trr2(b2r0 + 57344, b2r0 + 59392); SB();
    PIN(aM4); aM4 = mfma32(W0_0, __builtin_bit_cast(s8, A2_14_1), aM4); W1_1 = trr2(b2r1 + 57344, b2r1 + 59392); SB();
    PIN(aM1); aM1 = mfma32(W0_1, __builtin_bit_cast(s8, A2_14_0), aM1); W1_2 = trr2(b2r0 + 57856, b2r0 + 59904); SB();
    PIN(aM5); aM5 = mfma32(W0_1, __builtin_bit_cast(s8, A2_14_1), aM5); W1_3 = trr2(b2r1 + 57856, b2r1 + 59904); SB();
    LOAD_A2(A2_22_0, 22, 0);
    PIN(aM2); aM2 = mfma32(W0_2, __builtin_bit_cast(s8, A2_14_0), aM2);  SB();
    LOAD_A2(A2_22_1, 22, 1);
    PIN(aM6); aM6 = mfma32(W0_2, __builtin_bit_cast(s8, A2_14_1), aM6);  SB();
    PIN(aM3); aM3 = mfma32(W0_3, __builtin_bit_cast(s8, A2_14_0), aM3);  SB();
    PIN(aM7); aM7 = mfma32(W0_3, __builtin_bit_cast(s8, A2_14_1), aM7);  SB();
    WAIT_BAR(14);
    PIN(A2_15_0); PIN(A2_15_1);
    TOG(b2r0); TOG(b2r1);
    PIN(aM0); aM0 = mfma32(W1_0, __builtin_bit_cast(s8, A2_15_0), aM0); W0_0 = trr2(b2r0 + 0, b2r0 + 2048); SB();
    PIN(aM4); aM4 = mfma32(W1_0, __builtin_bit_cast(s8, A2_15_1), aM4); W0_1 = trr2(b2r1 + 0, b2r1 + 2048); SB();
    PIN(aM1); aM1 = mfma32(W1_1, __builtin_bit_cast(s8, A2_15_0), aM1); W0_2 = trr2(b2r0 + 512, b2r0 + 2560); SB();
    PIN(aM5); aM5 = mfma32(W1_1, __builtin_bit_cast(s8, A2_15_1), aM5); W0_3 = trr2(b2r1 + 512, b2r1 + 2560); SB();
    LOAD_A2(A2_23_0, 23, 0);
    PIN(aM2); aM2 = mfma32(W1_2, __builtin_bit_cast(s8, A2_15_0), aM2);  SB();
    LOAD_A2(A2_23_1, 23, 1);
    PIN(aM6); aM6 = mfma32(W1_2, __builtin_bit_cast(s8, A2_15_1), aM6);  SB();
    PIN(aM3); aM3 = mfma32(W1_3, __builtin_bit_cast(s8, A2_15_0), aM3);  SB();
    PIN(aM7); aM7 = mfma32(W1_3, __builtin_bit_cast(s8, A2_15_1), aM7);  SB();
    WAIT_VM(14);
    PIN(A2_16_0); PIN(A2_16_1);
    PIN(aM0); aM0 = mfma32(W0_0, __builtin_bit_cast(s8, A2_16_0), aM0); W1_0 = trr2(b2r0 + 8192, b2r0 + 10240); SB();
    PIN(aM4); aM4 = mfma32(W0_0, __builtin_bit_cast(s8, A2_16_1), aM4); W1_1 = trr2(b2r1 + 8192, b2r1 + 10240); SB();
    PIN(aM1); aM1 = mfma32(W0_1, __builtin_bit_cast(s8, A2_16_0), aM1); W1_2 = trr2(b2r0 + 8704, b2r0 + 10752); SB();
    PIN(aM5); aM5 = mfma32(W0_1, __builtin_bit_cast(s8, A2_16_1), aM5); W1_3 = trr2(b2r1 + 8704, b2r1 + 10752); SB();
    LOAD_A2(A2_24_0, 24, 0);
    PIN(aM2); aM2 = mfma32(W0_2, __builtin_bit_cast(s8, A2_16_0), aM2);  SB();
    LOAD_A2(A2_24_1, 24, 1);
    PIN(aM6); aM6 = mfma32(W0_2, __builtin_bit_cast(s8, A2_16_1), aM6);  SB();
    PIN(aM3); aM3 = mfma32(W0_3, __builtin_bit_cast(s8, A2_16_0), aM3);  SB();
    PIN(aM7); aM7 = mfma32(W0_3, __builtin_bit_cast(s8, A2_16_1), aM7);  SB();
    WAIT_VM(14);
    PIN(A2_17_0); PIN(A2_17_1);
    PIN(aM0); aM0 = mfma32(W1_0, __builtin_bit_cast(s8, A2_17_0), aM0); W0_0 = trr2(b2r0 + 16384, b2r0 + 18432); SB();
    PIN(aM4); aM4 = mfma32(W1_0, __builtin_bit_cast(s8, A2_17_1), aM4); W0_1 = trr2(b2r1 + 16384, b2r1 + 18432); SB();
    PIN(aM1); aM1 = mfma32(W1_1, __builtin_bit_cast(s8, A2_17_0), aM1); W0_2 = trr2(b2r0 + 16896, b2r0 + 18944); SB();
    PIN(aM5); aM5 = mfma32(W1_1, __builtin_bit_cast(s8, A2_17_1), aM5); W0_3 = trr2(b2r1 + 16896, b2r1 + 18944); SB();
    LOAD_A2(A2_25_0, 25, 0);
    PIN(aM2); aM2 = mfma32(W1_2, __builtin_bit_cast(s8, A2_17_0), aM2);  SB();
    LOAD_A2(A2_25_1, 25, 1);
    PIN(aM6); aM6 = mfma32(W1_2, __builtin_bit_cast(s8, A2_17_1), aM6);  SB();
    PIN(aM3); aM3 = mfma32(W1_3, __builtin_bit_cast(s8, A2_17_0), aM3);  SB();
    PIN(aM7); aM7 = mfma32(W1_3, __builtin_bit_cast(s8, A2_17_1), aM7);  SB();
    WAIT_VM(14);
    PIN(A2_18_0); PIN(A2_18_1);
    PIN(aM0); aM0 = mfma32(W0_0, __builtin_bit_cast(s8, A2_18_0), aM0); W1_0 = trr2(b2r0 + 24576, b2r0 + 26624); SB();
    PIN(aM4); aM4 = mfma32(W0_0, __builtin_bit_cast(s8, A2_18_1), aM4); W1_1 = trr2(b2r1 + 24576, b2r1 + 26624); SB();
    PIN(aM1); aM1 = mfma32(W0_1, __builtin_bit_cast(s8, A2_18_0), aM1); W1_2 = trr2(b2r0 + 25088, b2r0 + 27136); SB();
    PIN(aM5); aM5 = mfma32(W0_1, __builtin_bit_cast(s8, A2_18_1), aM5); W1_3 = trr2(b2r1 + 25088, b2r1 + 27136); SB();
    LOAD_A2(A2_26_0, 26, 0);
    PIN(aM2); aM2 = mfma32(W0_2, __builtin_bit_cast(s8, A2_18_0), aM2);  SB();
    LOAD_A2(A2_26_1, 26, 1);
    PIN(aM6); aM6 = mfma32(W0_2, __builtin_bit_cast(s8, A2_18_1), aM6);  SB();
    PIN(aM3); aM3 = mfma32(W0_3, __builtin_bit_cast(s8, A2_18_0), aM3);  SB();
    PIN(aM7); aM7 = mfma32(W0_3, __builtin_bit_cast(s8, A2_18_1), aM7);  SB();
    WAIT_BAR(14);
    PIN(A2_19_0); PIN(A2_19_1);
    PIN(aM0); aM0 = mfma32(W1_0, __builtin_bit_cast(s8, A2_19_0), aM0); W0_0 = trr2(b2r0 + 32768, b2r0 + 34816); SB();
    PIN(aM4); aM4 = mfma32(W1_0, __builtin_bit_cast(s8, A2_19_1), aM4); W0_1 = trr2(b2r1 + 32768, b2r1 + 34816); SB();
    PIN(aM1); aM1 = mfma32(W1_1, __builtin_bit_cast(s8, A2_19_0), aM1); W0_2 = trr2(b2r0 + 33280, b2r0 + 35328); SB();
    PIN(aM5); aM5 = mfma32(W1_1, __builtin_bit_cast(s8, A2_19_1), aM5); W0_3 = trr2(b2r1 + 33280, b2r1 + 35328); SB();
    LOAD_A2(A2_27_0, 27, 0);
    PIN(aM2); aM2 = mfma32(W1_2, __builtin_bit_cast(s8, A2_19_0), aM2);  SB();
    LOAD_A2(A2_27_1, 27, 1);
    PIN(aM6); aM6 = mfma32(W1_2, __builtin_bit_cast(s8, A2_19_1), aM6);  SB();
    PIN(aM3); aM3 = mfma32(W1_3, __builtin_bit_cast(s8, A2_19_0), aM3);  SB();
    PIN(aM7); aM7 = mfma32(W1_3, __builtin_bit_cast(s8, A2_19_1), aM7);  SB();
    WAIT_VM(14);
    PIN(A2_20_0); PIN(A2_20_1);
    PIN(aM0); aM0 = mfma32(W0_0, __builtin_bit_cast(s8, A2_20_0), aM0); W1_0 = trr2(b2r0 + 40960, b2r0 + 43008); SB();
    PIN(aM4); aM4 = mfma32(W0_0, __builtin_bit_cast(s8, A2_20_1), aM4); W1_1 = trr2(b2r1 + 40960, b2r1 + 43008); SB();
    PIN(aM1); aM1 = mfma32(W0_1, __builtin_bit_cast(s8, A2_20_0), aM1); W1_2 = trr2(b2r0 + 41472, b2r0 + 43520); SB();
    PIN(aM5); aM5 = mfma32(W0_1, __builtin_bit_cast(s8, A2_20_1), aM5); W1_3 = trr2(b2r1 + 41472, b2r1 + 43520); SB();
    PIN(aM2); aM2 = mfma32(W0_2, __builtin_bit_cast(s8, A2_20_0), aM2);  SB();
    PIN(aM6); aM6 = mfma32(W0_2, __builtin_bit_cast(s8, A2_20_1), aM6);  SB();
    PIN(aM3); aM3 = mfma32(W0_3, __builtin_bit_cast(s8, A2_20_0), aM3);  SB();
    PIN(aM7); aM7 = mfma32(W0_3, __builtin_bit_cast(s8, A2_20_1), aM7);  SB();
    WAIT_VM(12);
    LDS_SPIN(L_CNT + 20);
    PIN(A2_21_0); PIN(A2_21_1);
    PIN(aM0); aM0 = mfma32(W1_0, __builtin_bit_cast(s8, A2_21_0), aM0); W0_0 = trr2(b2r0 + 49152, b2r0 + 51200); SB();
    PIN(aM4); aM4 = mfma32(W1_0, __builtin_bit_cast(s8, A2_21_1), aM4); W0_1 = trr2(b2r1 + 49152, b2r1 + 51200); SB();
    PIN(aM1); aM1 = mfma32(W1_1, __builtin_bit_cast(s8, A2_21_0), aM1); W0_2 = trr2(b2r0 + 49664, b2r0 + 51712); SB();
    PIN(aM5); aM5 = mfma32(W1_1, __builtin_bit_cast(s8, A2_21_1), aM5); W0_3 = trr2(b2r1 + 49664, b2r1 + 51712); SB();
    LOAD_A2(A2_28_0, 28, 0);
    PIN(aM2); aM2 = mfma32(W1_2, __builtin_bit_cast(s8, A2_21_0), aM2);  SB();
    LOAD_A2(A2_28_1, 28, 1);
    PIN(aM6); aM6 = mfma32(W1_2, __builtin_bit_cast(s8, A2_21_1), aM6);  SB();
    LOAD_A2(A2_29_0, 29, 0);
    PIN(aM3); aM3 = mfma32(W1_3, __builtin_bit_cast(s8, A2_21_0), aM3);  SB();
    LOAD_A2(A2_29_1, 29, 1);
    PIN(aM7); aM7 = mfma32(W1_3, __builtin_bit_cast(s8, A2_21_1), aM7);  SB();
    WAIT_VM(14);
    PIN(A2_22_0); PIN(A2_22_1);
    PIN(aM0); aM0 = mfma32(W0_0, __builtin_bit_cast(s8, A2_22_0), aM0); W1_0 = trr2(b2r0 + 57344, b2r0 + 59392); SB();
    PIN(aM4); aM4 = mfma32(W0_0, __builtin_bit_cast(s8, A2_22_1), aM4); W1_1 = trr2(b2r1 + 57344, b2r1 + 59392); SB();
    PIN(aM1); aM1 = mfma32(W0_1, __builtin_bit_cast(s8, A2_22_0), aM1); W1_2 = trr2(b2r0 + 57856, b2r0 + 59904); SB();
    PIN(aM5); aM5 = mfma32(W0_1, __builtin_bit_cast(s8, A2_22_1), aM5); W1_3 = trr2(b2r1 + 57856, b2r1 + 59904); SB();
    LOAD_A2(A2_30_0, 30, 0);
    PIN(aM2); aM2 = mfma32(W0_2, __builtin_bit_cast(s8, A2_22_0), aM2);  SB();
    LOAD_A2(A2_30_1, 30, 1);
    PIN(aM6); aM6 = mfma32(W0_2, __builtin_bit_cast(s8, A2_22_1), aM6);  SB();
    PIN(aM3); aM3 = mfma32(W0_3, __builtin_bit_cast(s8, A2_22_0), aM3);  SB();
    PIN(aM7); aM7 = mfma32(W0_3, __builtin_bit_cast(s8, A2_22_1), aM7);  SB();
    WAIT_BAR(14);
    PIN(A2_23_0); PIN(A2_23_1);
    TOG(b2r0); TOG(b2r1);
    PIN(aM0); aM0 = mfma32(W1_0, __builtin_bit_cast(s8, A2_23_0), aM0); W0_0 = trr2(b2r0 + 0, b2r0 + 2048); SB();
    PIN(aM4); aM4 = mfma32(W1_0, __builtin_bit_cast(s8, A2_23_1), aM4); W0_1 = trr2(b2r1 + 0, b2r1 + 2048); SB();
    PIN(aM1); aM1 = mfma32(W1_1, __builtin_bit_cast(s8, A2_23_0), aM1); W0_2 = trr2(b2r0 + 512, b2r0 + 2560); SB();
    PIN(aM5); aM5 = mfma32(W1_1, __builtin_bit_cast(s8, A2_23_1), aM5); W0_3 = trr2(b2r1 + 512, b2r1 + 2560); SB();
    LOAD_A2(A2_31_0, 31, 0);
    PIN(aM2); aM2 = mfma32(W1_2, __builtin_bit_cast(s8, A2_23_0), aM2);  SB();
    LOAD_A2(A2_31_1, 31, 1);
    PIN(aM6); aM6 = mfma32(W1_2, __builtin_bit_cast(s8, A2_23_1), aM6);  SB();
    PIN(aM3); aM3 = mfma32(W1_3, __builtin_bit_cast(s8, A2_23_0), aM3);  SB();
    PIN(aM7); aM7 = mfma32(W1_3, __builtin_bit_cast(s8, A2_23_1), aM7);  SB();
    WAIT_VM(14);
    PIN(A2_24_0); PIN(A2_24_1);
    PIN(aM0); aM0 = mfma32(W0_0, __builtin_bit_cast(s8, A2_24_0), aM0); W1_0 = trr2(b2r0 + 8192, b2r0 + 10240); SB();
    PIN(aM4); aM4 = mfma32(W0_0, __builtin_bit_cast(s8, A2_24_1), aM4); W1_1 = trr2(b2r1 + 8192, b2r1 + 10240); SB();
    PIN(aM1); aM1 = mfma32(W0_1, __builtin_bit_cast(s8, A2_24_0), aM1); W1_2 = trr2(b2r0 + 8704, b2r0 + 10752); SB();
    PIN(aM5); aM5 = mfma32(W0_1, __builtin_bit_cast(s8, A2_24_1), aM5); W1_3 = trr2(b2r1 + 8704, b2r1 + 10752); SB();
    LOAD_A2(A2_32_0, 32, 0);
    PIN(aM2); aM2 = mfma32(W0_2, __builtin_bit_cast(s8, A2_24_0), aM2);  SB();
    LOAD_A2(A2_32_1, 32, 1);
    PIN(aM6); aM6 = mfma32(W0_2, __builtin_bit_cast(s8, A2_24_1), aM6);  SB();
    PIN(aM3); aM3 = mfma32(W0_3, __builtin_bit_cast(s8, A2_24_0), aM3);  SB();
    PIN(aM7); aM7 = mfma32(W0_3, __builtin_bit_cast(s8, A2_24_1), aM7);  SB();
    WAIT_VM(14);
    PIN(A2_25_0); PIN(A2_25_1);
    PIN(aM0); aM0 = mfma32(W1_0, __builtin_bit_cast(s8, A2_25_0), aM0); W0_0 = trr2(b2r0 + 16384, b2r0 + 18432); SB();
    PIN(aM4); aM4 = mfma32(W1_0, __builtin_bit_cast(s8, A2_25_1), aM4); W0_1 = trr2(b2r1 + 16384, b2r1 + 18432); SB();
    PIN(aM1); aM1 = mfma32(W1_1, __builtin_bit_cast(s8, A2_25_0), aM1); W0_2 = trr2(b2r0 + 16896, b2r0 + 18944); SB();
    PIN(aM5); aM5 = mfma32(W1_1, __builtin_bit_cast(s8, A2_25_1), aM5); W0_3 = trr2(b2r1 + 16896, b2r1 + 18944); SB();
    LOAD_A2(A2_33_0, 33, 0);
    PIN(aM2); aM2 = mfma32(W1_2, __builtin_bit_cast(s8, A2_25_0), aM2);  SB();
    LOAD_A2(A2_33_1, 33, 1);
    PIN(aM6); aM6 = mfma32(W1_2, __builtin_bit_cast(s8, A2_25_1), aM6);  SB();
    PIN(aM3); aM3 = mfma32(W1_3, __builtin_bit_cast(s8, A2_25_0), aM3);  SB();
    PIN(aM7); aM7 = mfma32(W1_3, __builtin_bit_cast(s8, A2_25_1), aM7);  SB();
    WAIT_VM(14);
    PIN(A2_26_0); PIN(A2_26_1);
    PIN(aM0); aM0 = mfma32(W0_0, __builtin_bit_cast(s8, A2_26_0), aM0); W1_0 = trr2(b2r0 + 24576, b2r0 + 26624); SB();
    PIN(aM4); aM4 = mfma32(W0_0, __builtin_bit_cast(s8, A2_26_1), aM4); W1_1 = trr2(b2r1 + 24576, b2r1 + 26624); SB();
    PIN(aM1); aM1 = mfma32(W0_1, __builtin_bit_cast(s8, A2_26_0), aM1); W1_2 = trr2(b2r0 + 25088, b2r0 + 27136); SB();
    PIN(aM5); aM5 = mfma32(W0_1, __builtin_bit_cast(s8, A2_26_1), aM5); W1_3 = trr2(b2r1 + 25088, b2r1 + 27136); SB();
    LOAD_A2(A2_34_0, 34, 0);
    PIN(aM2); aM2 = mfma32(W0_2, __builtin_bit_cast(s8, A2_26_0), aM2);  SB();
    LOAD_A2(A2_34_1, 34, 1);
    PIN(aM6); aM6 = mfma32(W0_2, __builtin_bit_cast(s8, A2_26_1), aM6);  SB();
    PIN(aM3); aM3 = mfma32(W0_3, __builtin_bit_cast(s8, A2_26_0), aM3);  SB();
    PIN(aM7); aM7 = mfma32(W0_3, __builtin_bit_cast(s8, A2_26_1), aM7);  SB();
    WAIT_BAR(14);
    PIN(A2_27_0); PIN(A2_27_1);
    PIN(aM0); aM0 = mfma32(W1_0, __builtin_bit_cast(s8, A2_27_0), aM0); W0_0 = trr2(b2r0 + 32768, b2r0 + 34816); SB();
    PIN(aM4); aM4 = mfma32(W1_0, __builtin_bit_cast(s8, A2_27_1), aM4); W0_1 = trr2(b2r1 + 32768, b2r1 + 34816); SB();
    PIN(aM1); aM1 = mfma32(W1_1, __builtin_bit_cast(s8, A2_27_0), aM1); W0_2 = trr2(b2r0 + 33280, b2r0 + 35328); SB();
    PIN(aM5); aM5 = mfma32(W1_1, __builtin_bit_cast(s8, A2_27_1), aM5); W0_3 = trr2(b2r1 + 33280, b2r1 + 35328); SB();
    LOAD_A2(A2_35_0, 35, 0);
    PIN(aM2); aM2 = mfma32(W1_2, __builtin_bit_cast(s8, A2_27_0), aM2);  SB();
    LOAD_A2(A2_35_1, 35, 1);
    PIN(aM6); aM6 = mfma32(W1_2, __builtin_bit_cast(s8, A2_27_1), aM6);  SB();
    PIN(aM3); aM3 = mfma32(W1_3, __builtin_bit_cast(s8, A2_27_0), aM3);  SB();
    PIN(aM7); aM7 = mfma32(W1_3, __builtin_bit_cast(s8, A2_27_1), aM7);  SB();
    WAIT_VM(14);
    PIN(A2_28_0); PIN(A2_28_1);
    aS0 = mfma32(W0_0, __builtin_bit_cast(s8, A2_28_0), aS0); W1_0 = trr2(b2r0 + 40960, b2r0 + 43008); SB();
    aS4 = mfma32(W0_0, __builtin_bit_cast(s8, A2_28_1), aS4); W1_1 = trr2(b2r1 + 40960, b2r1 + 43008); SB();
    aS1 = mfma32(W0_1, __builtin_bit_cast(s8, A2_28_0), aS1); W1_2 = trr2(b2r0 + 41472, b2r0 + 43520); SB();
    aS5 = mfma32(W0_1, __builtin_bit_cast(s8, A2_28_1), aS5); W1_3 = trr2(b2r1 + 41472, b2r1 + 43520); SB();
    aS2 = mfma32(W0_2, __builtin_bit_cast(s8, A2_28_0), aS2);  SB();
    aS6 = mfma32(W0_2, __builtin_bit_cast(s8, A2_28_1), aS6);  SB();
    aS3 = mfma32(W0_3, __builtin_bit_cast(s8, A2_28_0), aS3);  SB();
    aS7 = mfma32(W0_3, __builtin_bit_cast(s8, A2_28_1), aS7);  SB();
    WAIT_VM(12);
    PIN(A2_29_0); PIN(A2_29_1);
    PIN(aS0); aS0 = mfma32(W1_0, __builtin_bit_cast(s8, A2_29_0), aS0); W0_0 = trr2(b2r0 + 49152, b2r0 + 51200); SB();
    PIN(aS4); aS4 = mfma32(W1_0, __builtin_bit_cast(s8, A2_29_1), aS4); W0_1 = trr2(b2r1 + 49152, b2r1 + 51200); SB();
    PIN(aS1); aS1 = mfma32(W1_1, __builtin_bit_cast(s8, A2_29_0), aS1); W0_2 = trr2(b2r0 + 49664, b2r0 + 51712); SB();
    PIN(aS5); aS5 = mfma32(W1_1, __builtin_bit_cast(s8, A2_29_1), aS5); W0_3 = trr2(b2r1 + 49664, b2r1 + 51712); SB();
    PIN(aS2); aS2 = mfma32(W1_2, __builtin_bit_cast(s8, A2_29_0), aS2);  SB();
    PIN(aS6); aS6 = mfma32(W1_2, __builtin_bit_cast(s8, A2_29_1), aS6);  SB();
    PIN(aS3); aS3 = mfma32(W1_3, __builtin_bit_cast(s8, A2_29_0), aS3);  SB();
    PIN(aS7); aS7 = mfma32(W1_3, __builtin_bit_cast(s8, A2_29_1), aS7);  SB();
    SEND(aM);
    WAIT_VM(10);
    PIN(A2_30_0); PIN(A2_30_1);
    PIN(aS0); aS0 = mfma32(W0_0, __builtin_bit_cast(s8, A2_30_0), aS0); W1_0 = trr2(b2r0 + 57344, b2r0 + 59392); SB();
    PIN(aS4); aS4 = mfma32(W0_0, __builtin_bit_cast(s8, A2_30_1), aS4); W1_1 = trr2(b2r1 + 57344, b2r1 + 59392); SB();
    PIN(aS1); aS1 = mfma32(W0_1, __builtin_bit_cast(s8, A2_30_0), aS1); W1_2 = trr2(b2r0 + 57856, b2r0 + 59904); SB();
    PIN(aS5); aS5 = mfma32(W0_1, __builtin_bit_cast(s8, A2_30_1), aS5); W1_3 = trr2(b2r1 + 57856, b2r1 + 59904); SB();
    PIN(aS2); aS2 = mfma32(W0_2, __builtin_bit_cast(s8, A2_30_0), aS2);  SB();
    PIN(aS6); aS6 = mfma32(W0_2, __builtin_bit_cast(s8, A2_30_1), aS6);  SB();
    PIN(aS3); aS3 = mfma32(W0_3, __builtin_bit_cast(s8, A2_30_0), aS3);  SB();
    PIN(aS7); aS7 = mfma32(W0_3, __builtin_bit_cast(s8, A2_30_1), aS7);  SB();
    WAIT_BAR(8);
    PIN(A2_31_0); PIN(A2_31_1);
    TOG(b2r0); TOG(b2r1);
    PIN(aS0); aS0 = mfma32(W1_0, __builtin_bit_cast(s8, A2_31_0), aS0); W0_0 = trr2(b2r0 + 0, b2r0 + 2048); SB();
    PIN(aS4); aS4 = mfma32(W1_0, __builtin_bit_cast(s8, A2_31_1), aS4); W0_1 = trr2(b2r1 + 0, b2r1 + 2048); SB();
    PIN(aS1); aS1 = mfma32(W1_1, __builtin_bit_cast(s8, A2_31_0), aS1); W0_2 = trr2(b2r0 + 512, b2r0 + 2560); SB();
    PIN(aS5); aS5 = mfma32(W1_1, __builtin_bit_cast(s8, A2_31_1), aS5); W0_3 = trr2(b2r1 + 512, b2r1 + 2560); SB();
    PIN(aS2); aS2 = mfma32(W1_2, __builtin_bit_cast(s8, A2_31_0), aS2);  SB();
    PIN(aS6); aS6 = mfma32(W1_2, __builtin_bit_cast(s8, A2_31_1), aS6);  SB();
    PIN(aS3); aS3 = mfma32(W1_3, __builtin_bit_cast(s8, A2_31_0), aS3);  SB();
    PIN(aS7); aS7 = mfma32(W1_3, __builtin_bit_cast(s8, A2_31_1), aS7);  SB();
    WAIT_VM(6);
    PIN(A2_32_0); PIN(A2_32_1);
    PIN(aS0); aS0 = mfma32(W0_0, __builtin_bit_cast(s8, A2_32_0), aS0); W1_0 = trr2(b2r0 + 8192, b2r0 + 10240); SB();
    PIN(aS4); aS4 = mfma32(W0_0, __builtin_bit_cast(s8, A2_32_1), aS4); W1_1 = trr2(b2r1 + 8192, b2r1 + 10240); SB();
    PIN(aS1); aS1 = mfma32(W0_1, __builtin_bit_cast(s8, A2_32_0), aS1); W1_2 = trr2(b2r0 + 8704, b2r0 + 10752); SB();
    PIN(aS5); aS5 = mfma32(W0_1, __builtin_bit_cast(s8, A2_32_1), aS5); W1_3 = trr2(b2r1 + 8704, b2r1 + 10752); SB();
    PIN(aS2); aS2 = mfma32(W0_2, __builtin_bit_cast(s8, A2_32_0), aS2);  SB();
    PIN(aS6); aS6 = mfma32(W0_2, __builtin_bit_cast(s8, A2_32_1), aS6);  SB();
    PIN(aS3); aS3 = mfma32(W0_3, __builtin_bit_cast(s8, A2_32_0), aS3);  SB();
    PIN(aS7); aS7 = mfma32(W0_3, __builtin_bit_cast(s8, A2_32_1), aS7);  SB();
    WAIT_VM(4);
    PIN(A2_33_0); PIN(A2_33_1);
    PIN(aS0); aS0 = mfma32(W1_0, __builtin_bit_cast(s8, A2_33_0), aS0); W0_0 = trr2(b2r0 + 16384, b2r0 + 18432); SB();
    PIN(aS4); aS4 = mfma32(W1_0, __builtin_bit_cast(s8, A2_33_1), aS4); W0_1 = trr2(b2r1 + 16384, b2r1 + 18432); SB();
    PIN(aS1); aS1 = mfma32(W1_1, __builtin_bit_cast(s8, A2_33_0), aS1); W0_2 = trr2(b2r0 + 16896, b2r0 + 18944); SB();
    PIN(aS5); aS5 = mfma32(W1_1, __builtin_bit_cast(s8, A2_33_1), aS5); W0_3 = trr2(b2r1 + 16896, b2r1 + 18944); SB();
    PIN(aS2); aS2 = mfma32(W1_2, __builtin_bit_cast(s8, A2_33_0), aS2);  SB();
    PIN(aS6); aS6 = mfma32(W1_2, __builtin_bit_cast(s8, A2_33_1), aS6);  SB();
    PIN(aS3); aS3 = mfma32(W1_3, __builtin_bit_cast(s8, A2_33_0), aS3);  SB();
    PIN(aS7); aS7 = mfma32(W1_3, __builtin_bit_cast(s8, A2_33_1), aS7);  SB();
    WAIT_VM(2);
    PIN(A2_34_0); PIN(A2_34_1);
    PIN(aS0); aS0 = mfma32(W0_0, __builtin_bit_cast(s8, A2_34_0), aS0); W1_0 = trr2(b2r0 + 24576, b2r0 + 26624); SB();
    PIN(aS4); aS4 = mfma32(W0_0, __builtin_bit_cast(s8, A2_34_1), aS4); W1_1 = trr2(b2r1 + 24576, b2r1 + 26624); SB();
    PIN(aS1); aS1 = mfma32(W0_1, __builtin_bit_cast(s8, A2_34_0), aS1); W1_2 = trr2(b2r0 + 25088, b2r0 + 27136); SB();
    PIN(aS5); aS5 = mfma32(W0_1, __builtin_bit_cast(s8, A2_34_1), aS5); W1_3 = trr2(b2r1 + 25088, b2r1 + 27136); SB();
    PIN(aS2); aS2 = mfma32(W0_2, __builtin_bit_cast(s8, A2_34_0), aS2);  SB();
    PIN(aS6); aS6 = mfma32(W0_2, __builtin_bit_cast(s8, A2_34_1), aS6);  SB();
    PIN(aS3); aS3 = mfma32(W0_3, __builtin_bit_cast(s8, A2_34_0), aS3);  SB();
    PIN(aS7); aS7 = mfma32(W0_3, __builtin_bit_cast(s8, A2_34_1), aS7);  SB();
    WAIT_VM(0);
    PIN(A2_35_0); PIN(A2_35_1);
    PIN(aS0); aS0 = mfma32(W1_0, __builtin_bit_cast(s8, A2_35_0), aS0);  SB();
    PIN(aS4); aS4 = mfma32(W1_0, __builtin_bit_cast(s8, A2_35_1), aS4);  SB();
    PIN(aS1); aS1 = mfma32(W1_1, __builtin_bit_cast(s8, A2_35_0), aS1);  SB();
    PIN(aS5); aS5 = mfma32(W1_1, __builtin_bit_cast(s8, A2_35_1), aS5);  SB();
    PIN(aS2); aS2 = mfma32(W1_2, __builtin_bit_cast(s8, A2_35_0), aS2);  SB();
    PIN(aS6); aS6 = mfma32(W1_2, __builtin_bit_cast(s8, A2_35_1), aS6);  SB();
    PIN(aS3); aS3 = mfma32(W1_3, __builtin_bit_cast(s8, A2_35_0), aS3);  SB();
    PIN(aS7); aS7 = mfma32(W1_3, __builtin_bit_cast(s8, A2_35_1), aS7);  SB();
    { const unsigned sbuf = sm0 + L_RED2 + (2 * (w & 1)) * 8192 + ((w >> 1) & 1) * 4096 + l16; SEND(aS); }
    LDS_BAR();
  }
  if (isld) {
    __builtin_amdgcn_s_setprio(0);
    f4 gt[4];
    WAIT_VM(0);
    ARRIVE2();
    LDS_BAR();
    FILL2_B2(16);
    FILL2_B2(17);
    FILL2_B2(18);
    FILL2_B2(19);
    WAIT_BAR(8);
    if (w == 4) { poll_slow(ws, WS_F2 + pofs, tag); *(volatile LAS unsigned*)(sm0 + L_CNT + 16) = 1u; }
    FILL2_B2(20);
    FILL2_B2(21);
    FILL2_B2(22);
    FILL2_B2(23);
    WAIT_BAR(16);
    FILL2_B2(24);
    FILL2_B2(25);
    FILL2_B2(26);
    FILL2_B2(27);
    WAIT_BAR(16);
    FILL2_B2(28);
    FILL2_B2(29);
    FILL2_B2(30);
    FILL2_B2(31);
    WAIT_BAR(16);
    if (w == 4) { poll_slow(ws, WS_FO + pofs, tag); *(volatile LAS unsigned*)(sm0 + L_CNT + 20) = 1u; }
    FILL2_B2(32);
    FILL2_B2(33);
    FILL2_B2(34);
    FILL2_B2(35);
    WAIT_BAR(16);
    GLDS(voffX2, xt, wb, L_XR); GLDS(voffX2, xt - 32768, wb, L_XR - 4096);
    WAIT_BAR(0);
    EPOCH_BUMP();
    WAIT_BAR(0);
    GATE_CALC();
    const unsigned xa = sm0 + L_XR + (16 * rg + r) * 128 + 8 * (g & 1);
    u2 xr[4];
#pragma unroll
    for (int t = 0; t < 4; ++t) xr[t] = *(LAS u2*)(xa + (((2 * t + (g >> 1)) ^ (r >> 1)) << 4));
    LDS_BAR();
#pragma unroll
    for (int t = 0; t < 4; ++t) {
      const f4 sv = ldsrf4(rbuf + (L_RED2 - L_RED) + t * 1024) + ldsrf4(rbuf + (L_RED2 - L_RED) + 4096 + t * 1024);
      UNPK(xv, xr[t]);
      u2 ov = {pk2(__builtin_fmaf(sv[0], gt[t][0], xv[0]), __builtin_fmaf(sv[1], gt[t][1], xv[1])), pk2(__builtin_fmaf(sv[2], gt[t][2], xv[2]), __builtin_fmaf(sv[3], gt[t][3], xv[3]))};
      *(LAS u2*)(xa + (((2 * t + (g >> 1)) ^ (r >> 1)) << 4)) = ov;
    }
  }
  LDS_BAR();
  {
    u4 ot = *(LAS u4*)(wb + L_XR + l16);
    asm volatile("global_store_dwordx4 %0, %1, %2 sc1\n\ts_nop 1" :: "v"(voffX2), "v"(ot), "s"(out + (size_t)rt64 * 65536 + ct * 128) : "memory");
  }
}

extern "C" void kernel_launch(void* const* d_in, const int* in_sizes, int n_in, void* d_out, int out_size,
                              void* d_ws, size_t ws_size, hipStream_t stream) {
  fused_kernel<<<256, 512, 0, stream>>>((const char*)d_in[0], (const char*)d_in[1], (const char*)d_in[2],
                                        (const char*)d_in[3], (const char*)d_in[5], (const char*)d_in[6],
                                        (const char*)d_in[7], (const char*)d_in[8], (char*)d_out, (char*)d_ws);
}
```
